```python
import torch

def _softmax(x, axis=-1):
    return torch.softmax(x, dim=axis)
def _gelu(x):
    return torch.nn.functional.gelu(x, approximate="tanh")
def _silu(x):
    return torch.nn.functional.silu(x)
def _sigmoid(x):
    return torch.sigmoid(x)
def _layernorm(x, axis=-1, eps=1e-5):
    m = x.mean(dim=axis, keepdim=True)
    v = x.var(dim=axis, keepdim=True, unbiased=False)
    return (x - m) / torch.sqrt(v + eps)
def _scatter_add(tgt, idx, src, axis=0):
    return tgt.clone().index_add_(axis, idx, src)
def _segment_sum(src, seg, n):
    out = torch.zeros((n,) + src.shape[1:], dtype=src.dtype)
    return out.index_add_(0, seg, src)
def _pad(x, pw):
    flat = tuple(v for pair in reversed(pw) for v in pair)
    return torch.nn.functional.pad(x, flat)

kernel_name = 'sequential_preLN__sliding_block_cached__glu_gated_swiglu__decode'

def setup_inputs():

    _w = (lambda a: (lambda t: t.to(torch.bfloat16) if t.is_floating_point() else t)(torch.as_tensor(a)))
    def _A_setup():
        B, Sq, D = 1, 1, 640
        Hq, Hkv, Sk, Dh = 32, 32, 32768, 192
        assert Hq % Hkv == 0, (Hq, Hkv)
        return {
            "x": _w((lambda *s: torch.randn(*s, dtype=torch.bfloat16) * 0.05)(B, Sq, D)),
            "k_cache": _w((lambda *s: torch.randn(*s, dtype=torch.bfloat16) * 1.6)(B, Hkv, Sk, Dh)),
            "v_cache": _w((lambda *s: torch.randn(*s, dtype=torch.bfloat16) * 0.8)(B, Hkv, Sk, Dh)),
            "wq": _w((lambda *s: torch.randn(*s, dtype=torch.bfloat16) * 1.6)(D, Hq * Dh) * (D ** -0.5)),
            "wo": _w((lambda *s: torch.randn(*s, dtype=torch.bfloat16) * 0.8)(Hq * Dh, D) * ((Hq * Dh) ** -0.5)),
        }
    _A_in = _A_setup()

    def _B_setup():
        B, S, D, F = 1, 1, 640, 1536
        return {
            "x": _w((lambda *s: torch.randn(*s, dtype=torch.bfloat16) * 0.05)(B, S, D)),
            "w1": _w((lambda *s: torch.randn(*s, dtype=torch.bfloat16) * 0.4)(D, F) * (2.0 / D) ** 0.5),
            "w3": _w((lambda *s: torch.randn(*s, dtype=torch.bfloat16) * 0.4)(D, F) * (2.0 / D) ** 0.5),
            "w2": _w((lambda *s: torch.randn(*s, dtype=torch.bfloat16) * 0.4)(F, D) * (2.0 / F) ** 0.5),
        }
    _B_in = _B_setup()

    return {
        "A_x": _A_in["x"],
        "A_k_cache": _A_in["k_cache"],
        "A_v_cache": _A_in["v_cache"],
        "A_wq": _A_in["wq"],
        "A_wo": _A_in["wo"],
        "B_x": _B_in["x"],
        "B_w1": _B_in["w1"],
        "B_w3": _B_in["w3"],
        "B_w2": _B_in["w2"],
    }

def reference(A_x, A_k_cache, A_v_cache, A_wq, A_wo, B_x, B_w1, B_w3, B_w2):
    def _A_ref(x, k_cache, v_cache, wq, wo):
        B, Sq, D = x.shape
        Hq, Dh = 32, 192
        Hkv, Sk = k_cache.shape[1], k_cache.shape[2]
        W = min(4096, max(256, Sk // 4))
        rep = Hq // Hkv
        q = (lambda x, *d: x.permute(*d))((x @ wq).reshape(B, Sq, Hq, Dh), 0, 2, 1, 3)
        k = (lambda x, n, axis: torch.repeat_interleave(x, n, dim=axis))(k_cache[:, :, Sk - W:, :], rep, axis=1)
        v = (lambda x, n, axis: torch.repeat_interleave(x, n, dim=axis))(v_cache[:, :, Sk - W:, :], rep, axis=1)
        scores = torch.einsum("bhqd,bhkd->bhqk", q, k) * (Dh ** -0.5)
        p = _softmax(scores, axis=-1)
        o = torch.einsum("bhqk,bhkd->bhqd", p, v)
        return (lambda x, *d: x.permute(*d))(o, 0, 2, 1, 3).reshape(B, Sq, Hq * Dh) @ wo

    def _B_ref(x, w1, w3, w2):
        gate = torch.einsum("bsd,df->bsf", x, w1)
        up = torch.einsum("bsd,df->bsf", x, w3)
        h = _silu(gate) * up
        out = torch.einsum("bsf,fd->bsd", h, w2)
        return out

    x = A_x
    h = x + _A_ref(_layernorm(x, axis=-1), A_k_cache, A_v_cache, A_wq, A_wo)
    h2 = h + _B_ref(_layernorm(h, axis=-1), B_w1, B_w3, B_w2)
    return h2
```

```cpp
#define P0 105
#define KD 167
#define KDB 157
#define TB 146
#define FL 4615
#define TD 270
#define TY 589170025
#define TW 3888
#include <hip/hip_runtime.h>
#include <cstdint>

namespace {
constexpr int D = 640, H = 32, DH = 192, SK = 32768, WIN = 4096, F = 1536;
constexpr int NWG = 256, NTHR = 1024;
constexpr int N5 = 48, R5 = F / N5;
constexpr int NST = 16, NREC = 8;
constexpr int WS_PQ = 256;
constexpr int WS_TO = WS_PQ + 320 * 32 * 16;
constexpr int WS_P1 = WS_TO + 1024;
constexpr int WS_P2 = WS_P1 + 32 * NREC * 1024;
constexpr int WS_G = WS_P2 + 320 * 32 * 16;
constexpr int WS_ST = WS_G + N5 * 8 * 64 * 16;
constexpr int WS_P5 = WS_ST + 24 * 8 * 128;
constexpr int NG4 = 10, G3 = 0, G4 = G3 + N5, NGT = G4 + NG4 * 3;
constexpr int WS_GT = WS_P5 + 320 * N5 * 16;
constexpr int WS_END = WS_GT + NGT * 256;
constexpr int slot(int seg, int n) { return WS_GT + (seg + (n >> 4)) * 256 + (n & 15) * 4; }
constexpr int L_RING = 0;
constexpr int L_WO = 49152;
constexpr int L_FF = 2 * 49152;
constexpr int L_XS = L_FF, L_PART = L_FF + 2560;
constexpr int L_XRAW = 3 * 49152;
constexpr int L_P = L_XRAW + 1280;
constexpr int L_REC = L_P + 1024;
constexpr int L_QS = L_REC + 8 * 416;
constexpr int L_HS = L_QS + 384;
constexpr int L_ACT = L_HS + 384;
constexpr int L_WMAX = L_ACT + 80, L_LSUM = L_WMAX + 64;
constexpr int L_WRE = L_LSUM + 64;
constexpr int L_CTL = L_WRE + 64;
constexpr int L_END = L_CTL + 128;
static_assert(L_END <= 163840, "lds");
#define ADL 20
#define ALT 190
#define ALD 40
#define ADS 40
#define FSL 20
#define D4 66
constexpr unsigned SPIN_CAP = 1u << 18;
#define HOLD 30
#define PD 182
#define PDB 171
#define PDE 186
#define WA 0
#define WF 0
#define OM 12
#define W2E 4
#define F1D ((PAR(6, TY) >> 8) & 255)
#define F1S ((PAR(6, TY) >> 16) & 255)
#define W2T ((PAR(6, TY) >> 24) & 127)
#define TP 0
#define TL 36
#define WQD ((PAR(4, FL) >> 8) & 255)
#define MS 50
#define PW 10
#define RH 150
#define HS 2
#define GK (PAR(4, FL) >> 16)
#define QM 9
#define PAR(i, dflt) (dflt)
#define RTL()
constexpr int WS_STAMP = 1 << 22;
static_assert(WS_END <= WS_STAMP, "ws");
#define STAMP(i)
#define CNT(e) (void)(e)

typedef unsigned short u16;
typedef unsigned u32x2 __attribute__((ext_vector_type(2)));
typedef unsigned u32x4 __attribute__((ext_vector_type(4)));
typedef float f32x2 __attribute__((ext_vector_type(2)));
typedef float f32x4 __attribute__((ext_vector_type(4)));
typedef __attribute__((ext_vector_type(2))) __bf16 bf16x2;
typedef __amdgpu_buffer_rsrc_t rsrc_t;

__device__ __forceinline__ float lo16(unsigned u) { return __uint_as_float(u << 16); }
__device__ __forceinline__ float hi16(unsigned u) { return __uint_as_float(u & 0xFFFF0000u); }
__device__ __forceinline__ unsigned pk2(float a, float b) {
  f32x2 v = {a, b};
  return __builtin_bit_cast(unsigned, __builtin_convertvector(v, bf16x2));
}
__device__ __forceinline__ float bfr(float f) { return __uint_as_float(pk2(0.f, f)); }
__device__ __forceinline__ float dot2(unsigned a, unsigned b, float acc) {
  return __builtin_amdgcn_fdot2_f32_bf16(__builtin_bit_cast(bf16x2, a), __builtin_bit_cast(bf16x2, b), acc, false);
}
template <int CTRL> __device__ __forceinline__ float dppf(float x) {
  return __builtin_bit_cast(float, __builtin_amdgcn_mov_dpp(__builtin_bit_cast(int, x), CTRL, 0xf, 0xf, true));
}
constexpr int XOR1 = 0xB1, XOR2 = 0x4E, HMIR = 0x141, MIR = 0x140, ROR8 = 0x128;
__device__ __forceinline__ float sw16(float a, float b) {
  auto s = __builtin_amdgcn_permlane16_swap(__float_as_uint(a), __float_as_uint(b), false, false);
  return __uint_as_float(s[0]) + __uint_as_float(s[1]);
}
__device__ __forceinline__ float sw32(float a, float b) {
  auto s = __builtin_amdgcn_permlane32_swap(__float_as_uint(a), __float_as_uint(b), false, false);
  return __uint_as_float(s[0]) + __uint_as_float(s[1]);
}
__device__ __forceinline__ float red16(float x) {
  x += dppf<XOR1>(x); x += dppf<XOR2>(x); x += dppf<HMIR>(x); x += dppf<MIR>(x); return x;
}
__device__ __forceinline__ float red32(float x) { x = red16(x); return sw16(x, x); }
__device__ __forceinline__ float red64(float x) { x = red32(x); return sw32(x, x); }
__device__ __forceinline__ float max16(float x) {
  x = fmaxf(x, dppf<XOR1>(x)); x = fmaxf(x, dppf<XOR2>(x)); x = fmaxf(x, dppf<HMIR>(x)); x = fmaxf(x, dppf<MIR>(x)); return x;
}
__device__ __forceinline__ float max64(float x) {
  x = max16(x);
  auto s = __builtin_amdgcn_permlane16_swap(__float_as_uint(x), __float_as_uint(x), false, false);
  x = fmaxf(__uint_as_float(s[0]), __uint_as_float(s[1]));
  auto t = __builtin_amdgcn_permlane32_swap(__float_as_uint(x), __float_as_uint(x), false, false);
  return fmaxf(__uint_as_float(t[0]), __uint_as_float(t[1]));
}
template <int CTRL> __device__ __forceinline__ float rs(float a, float b, bool sel) {
  const float keep = sel ? b : a, send = sel ? a : b;
  return keep + dppf<CTRL>(send);
}
__device__ __forceinline__ unsigned plo(unsigned r0, unsigned r1) { return __builtin_amdgcn_perm(r1, r0, 0x05040100u); }
__device__ __forceinline__ unsigned phi(unsigned r0, unsigned r1) { return __builtin_amdgcn_perm(r1, r0, 0x07060302u); }
__device__ __forceinline__ rsrc_t mk(const void* p, int bytes) {
  return __builtin_amdgcn_make_buffer_rsrc(const_cast<void*>(p), 0, bytes, 0x00020000);
}
#define BAR() asm volatile("s_waitcnt lgkmcnt(0)\n\ts_barrier" ::: "memory")
#define PIN() __builtin_amdgcn_sched_barrier(0)
constexpr int SC1 = 16, NT = 2, OOB = 0x40000000;

template <int AUX = SC1>
__device__ __forceinline__ void put_unit(rsrc_t rw, int off, float v0, float v1, unsigned ta, unsigned tb) {
  u32x4 u = {__float_as_uint(v0), ta, __float_as_uint(v1), tb};
  __builtin_amdgcn_raw_buffer_store_b128(u, rw, off, 0, AUX);
}
template <int N, int STRIDE, int AUX = SC1>
__device__ __forceinline__ void load_units(rsrc_t rw, int voff, u32x4 (&v)[N]) {
#pragma unroll
  for (int k = 0; k < N; ++k) v[k] = __builtin_amdgcn_raw_buffer_load_b128(rw, voff, k * STRIDE, AUX);
}
template <int N>
__device__ __forceinline__ unsigned units_bad(const u32x4 (&v)[N], unsigned ta, unsigned tb, unsigned acc = 0u) {
  unsigned acc2 = 0u;
#pragma unroll
  for (int k = 0; k < N; ++k) { acc = __builtin_amdgcn_bitop3_b32(v[k].y, ta, acc, 0xBE); acc2 = __builtin_amdgcn_bitop3_b32(v[k].w, tb, acc2, 0xBE); }
  return acc | acc2;
}
__device__ __forceinline__ bool none(bool p) { return __builtin_amdgcn_ballot_w64(p) == 0ull; }
#define RARE(c) __builtin_expect(!!(c), 0)
template <int N, int STRIDE, int AUX = SC1>
__device__ __forceinline__ unsigned poll_units(rsrc_t rw, int voff, u32x4 (&v)[N], unsigned ta, unsigned tb, bool live = true) {
  unsigned spins = 0;
#pragma unroll 1
  for (; spins < SPIN_CAP; ++spins) {
    asm volatile("" : "+v"(voff) :: "memory");
    load_units<N, STRIDE, AUX>(rw, voff, v);
    if (none(units_bad<N>(v, ta, tb) != 0u && live)) break;
  }
  return spins;
}
__device__ __forceinline__ unsigned lds_ld(unsigned* p) {
  return __builtin_amdgcn_readfirstlane(__hip_atomic_load(p, __ATOMIC_RELAXED, __HIP_MEMORY_SCOPE_WORKGROUP));
}
typedef __attribute__((address_space(3))) unsigned* lds_u32;
__device__ __forceinline__ void lds_inc(unsigned* p, int lane, unsigned n = 1u) {
  asm volatile("s_waitcnt lgkmcnt(0)\n\ts_mov_b64 exec, 1\n\tds_add_u32 %0, %1\n\ts_mov_b64 exec, -1" :: "v"((unsigned)(size_t)(lds_u32)p), "v"(n) : "memory");
}
__device__ __forceinline__ void pv_pair(float (&a)[24], const u32x4* A, const u32x4* B, const unsigned (&xp)[3]) {
#pragma unroll
  for (int jj = 0; jj < 3; ++jj)
#pragma unroll
    for (int i = 0; i < 4; ++i) {
      a[8 * jj + 2 * i] = dot2(plo(A[jj][i], B[jj][i]), xp[jj], a[8 * jj + 2 * i]);
      a[8 * jj + 2 * i + 1] = dot2(phi(A[jj][i], B[jj][i]), xp[jj], a[8 * jj + 2 * i + 1]);
    }
}
__device__ __forceinline__ f32x4 red_rows(const float (&a)[24], bool t1) {
  float bq[12], v[6];
#pragma unroll
  for (int k = 0; k < 12; ++k) bq[k] = sw32(a[k], a[k + 12]);
#pragma unroll
  for (int k = 0; k < 6; ++k) { v[k] = sw16(bq[k], bq[k + 6]); v[k] += dppf<ROR8>(v[k]); }
  return f32x4{t1 ? v[4] : v[0], t1 ? v[5] : v[1], v[2], v[3]};
}
template <int N, int STRIDE>
__device__ __forceinline__ unsigned poll_lane(rsrc_t rw, int voff, u32x4 (&v)[N], unsigned ta, unsigned tb) {
  unsigned spins = 0;
#pragma unroll 1
  for (; spins < SPIN_CAP; ++spins) {
    if (none(units_bad<N>(v, ta, tb) != 0u)) break;
#pragma unroll
    for (int k = 0; k < N; ++k) {
      const unsigned long long em = __builtin_amdgcn_ballot_w64(((v[k].y ^ ta) | (v[k].w ^ tb)) != 0u);
      asm volatile("s_mov_b64 exec, %3\n\tbuffer_load_dwordx4 %0, %1, %2, %4 offen sc1\n\ts_mov_b64 exec, -1" : "+v"(v[k]) : "v"(voff), "s"(rw), "s"(em), "s"(k * STRIDE) : "memory");
    }
    if constexpr (N == 2) asm volatile("s_waitcnt vmcnt(0)" : "+v"(v[0]), "+v"(v[1]) :: "memory");
    else asm volatile("s_waitcnt vmcnt(0)" : "+v"(v[0]), "+v"(v[1]), "+v"(v[2]), "+v"(v[3]) :: "memory");
  }
  return spins;
}
#define SL_ "s_sleep 2\n\t"
#define RING(p, t, em) asm volatile("s_mov_b64 exec, %2\n\tglobal_atomic_swap %0, %1, off sc1\n\ts_mov_b64 exec, -1" :: "v"(p), "v"(t), "s"(em) : "memory")
__device__ __forceinline__ void wait_marks(const void* p, unsigned tg, unsigned long long em, unsigned (&s)[4]) {
  unsigned cnt = SPIN_CAP / 4;
#define A1_(a) "global_atomic_add %[" #a "], %[p], %[z], off sc0 sc1\n\t"
#define CH_(a) "s_waitcnt vmcnt(3)\n\tv_cmpx_ne_u32 exec, %[" #a "], %[tg]\n\ts_cbranch_execz 2f\n\t" SL_
  asm volatile(
      "s_mov_b64 exec, %[em]\n\t"
      A1_(a) SL_ A1_(b) SL_ A1_(c) SL_
      "1:\n\t"
      A1_(d) CH_(a)
      A1_(a) CH_(b)
      A1_(b) CH_(c)
      A1_(c) CH_(d)
      "s_sub_u32 %[cnt], %[cnt], 1\n\t"
      "s_cmp_lg_u32 %[cnt], 0\n\t"
      "s_cbranch_scc1 1b\n\t"
      "2:\n\t"
      "s_mov_b64 exec, -1"
      : [cnt] "+s"(cnt), [a] "=&v"(s[0]), [b] "=&v"(s[1]), [c] "=&v"(s[2]), [d] "=&v"(s[3])
      : [p] "v"(p), [z] "v"(0u), [tg] "v"(tg), [em] "s"(em)
      : "memory", "vcc", "scc");
}
__device__ __forceinline__ void announce(const void* p, unsigned tg, unsigned t, unsigned long long em) {
  asm volatile("s_mov_b64 exec, %2\n\tglobal_atomic_swap %0, %1, off sc1\n\ts_mov_b64 exec, -1" :: "v"(p), "v"((tg << 12) | (t & 0xFFFu)), "s"(em) : "memory");
}
__device__ __forceinline__ unsigned gate(const void* p, unsigned tg, unsigned long long em, unsigned (&s)[4]) {
  unsigned cnt = SPIN_CAP / 4, res = 0u, t;
#define CI_(a) "s_waitcnt vmcnt(3)\n\tv_mov_b32 %[res], %[" #a "]\n\tv_lshrrev_b32 %[t], 12, %[" #a "]\n\tv_cmpx_ne_u32 exec, %[t], %[tg]\n\ts_cbranch_execz 2f\n\ts_sleep 1\n\t"
  asm volatile(
      "s_mov_b64 exec, %[em]\n\t"
      A1_(a) SL_ A1_(b) SL_ A1_(c) SL_
      "1:\n\t"
      A1_(d) CI_(a)
      A1_(a) CI_(b)
      A1_(b) CI_(c)
      A1_(c) CI_(d)
      "s_sub_u32 %[cnt], %[cnt], 1\n\t"
      "s_cmp_lg_u32 %[cnt], 0\n\t"
      "s_cbranch_scc1 1b\n\t"
      "2:\n\t"
      "s_mov_b64 exec, -1"
      : [cnt] "+s"(cnt), [res] "+&v"(res), [t] "=&v"(t), [a] "=&v"(s[0]), [b] "=&v"(s[1]), [c] "=&v"(s[2]), [d] "=&v"(s[3])
      : [p] "v"(p), [z] "v"(0u), [tg] "v"(tg & 0xFFFFFu), [em] "s"(em)
      : "memory", "vcc", "scc");
  const unsigned now = (unsigned)__builtin_amdgcn_s_memrealtime();
  const float age = max64(-(float)(res ? (now - res) & 0xFFFu : 0xFFFu));
  return now + (unsigned)__builtin_amdgcn_readfirstlane((int)age);
}
__device__ __forceinline__ void until(unsigned due) {
  while (due - (unsigned)__builtin_amdgcn_s_memrealtime() - 1u < 400u) {}
}
#define KEEP(s) asm volatile("" :: "v"(s[0]), "v"(s[1]), "v"(s[2]), "v"(s[3]) : "memory")
typedef short s16x4 __attribute__((ext_vector_type(4)));
typedef __attribute__((ext_vector_type(8))) __bf16 bf16x8;
typedef __attribute__((address_space(3))) s16x4* lds_s16x4;
__device__ __forceinline__ u32x4 frag(unsigned a, int off, int off_hi) {
  const s16x4 lo = __builtin_amdgcn_ds_read_tr16_b64_v4i16((lds_s16x4)(size_t)(a + off));
  const s16x4 hi = __builtin_amdgcn_ds_read_tr16_b64_v4i16((lds_s16x4)(size_t)(a + off_hi));
  const u32x2 l2 = __builtin_bit_cast(u32x2, lo), h2 = __builtin_bit_cast(u32x2, hi);
  return u32x4{l2.x, l2.y, h2.x, h2.y};
}
__device__ __forceinline__ f32x4 mma(u32x4 a, u32x4 b, f32x4 c) {
  return __builtin_amdgcn_mfma_f32_16x16x32_bf16(__builtin_bit_cast(bf16x8, a), __builtin_bit_cast(bf16x8, b), c, 0, 0, 0);
}
typedef const __attribute__((address_space(3))) u32x4* lds_cv4;
__device__ __forceinline__ unsigned lds_addr(const void* p) { return (unsigned)(size_t)(const __attribute__((address_space(3))) void*)p; }
__device__ __forceinline__ u32x4 lds_ld4(unsigned a, int off) { return *(lds_cv4)(size_t)(a + off); }
__device__ __forceinline__ void lds_st(unsigned a, unsigned v) { *(lds_u32)(size_t)a = v; }
#define PINV(x) asm volatile("" : "+v"(x))
#define WAITC(i, n) while (lds_ld(&ctl[i]) < (n)) __builtin_amdgcn_s_sleep(1); asm volatile("" ::: "memory")
#define WAITS(i, n) while (lds_ld(&ctl[i]) < (n)) __builtin_amdgcn_s_sleep(4); asm volatile("" ::: "memory")
#define SPINC(i, n) while (lds_ld(&ctl[i]) < (n)) {} asm volatile("" ::: "memory")

__device__ __forceinline__ void dma1(rsrc_t r, int voff, int soff, unsigned lds) {
  asm volatile("s_mov_b32 m0, %0\n\ts_nop 0\n\tbuffer_load_dwordx4 %1, %2, %3 offen nt lds" :: "s"(lds), "v"(voff), "s"(r), "s"(soff) : "memory");
}
__device__ __forceinline__ void dma3(rsrc_t r, int voff, int soff, unsigned lds) {
  asm volatile("s_mov_b32 m0, %0\n\ts_nop 0\n\tbuffer_load_dwordx4 %1, %2, %3 offen nt lds\n\t"
               "s_add_u32 m0, m0, 896\n\ts_nop 0\n\tbuffer_load_dwordx4 %1, %2, %3 offen offset:128 nt lds\n\t"
               "s_add_u32 m0, m0, 896\n\ts_nop 0\n\tbuffer_load_dwordx4 %1, %2, %3 offen offset:256 nt lds" :: "s"(lds), "v"(voff), "s"(r), "s"(soff) : "memory", "scc");
}
#define VMW(n) asm volatile("s_waitcnt vmcnt(" #n ")" ::: "memory")
#define BAR0() asm volatile("s_barrier" ::: "memory")

__device__ __forceinline__ void code_ahead(int i) {
  asm volatile("s_getpc_b64 s[88:89]\n1:\n\t"
               "s_add_u32 s88, s88, .Lkf_begin - 1b\n\ts_addc_u32 s89, s89, -1\n\ts_and_b32 s89, s89, 0xffff\n\t"
               "s_mov_b32 s90, 24576\n\ts_mov_b32 s91, 0x20000\n\t"
               "s_buffer_load_dword s92, s[88:91], %0 offset:0\n\ts_buffer_load_dword s92, s[88:91], %0 offset:64\n\t"
               "s_buffer_load_dword s92, s[88:91], %0 offset:128\n\ts_buffer_load_dword s92, s[88:91], %0 offset:192\n\t"
               "s_buffer_load_dword s92, s[88:91], %0 offset:256\n\ts_buffer_load_dword s92, s[88:91], %0 offset:320\n\t"
               "s_waitcnt lgkmcnt(0)"
               :: "s"(i * 384) : "memory", "scc", "s88", "s89", "s90", "s91", "s92");
}
__global__ __launch_bounds__(NTHR, 4) void k_fused(const u16* x_, const u16* kc_, const u16* wq_, unsigned char* ws_, const u16* vc_, const u16* wo_,
                                                   const u16* w1_, const u16* w3_, const u16* w2_, u16* out_
) {
  typedef unsigned u32x8 __attribute__((ext_vector_type(8)));
  typedef __attribute__((address_space(1))) unsigned char* gp_t;
  const unsigned long long kap = (unsigned long long)__builtin_amdgcn_kernarg_segment_ptr();
  u32x8 ka, kb;
  u32x4 kc4;
  asm volatile(".Lkf_begin:\n\ts_load_dword s95, %0, 0x0" :: "s"(kap) : "memory", "s95");
#define ARGS() asm volatile("s_load_dwordx8 %0, %1, 0x0\n\ts_waitcnt lgkmcnt(0)" : "=&s"(ka) : "s"(kap) : "memory", "s95")
#define ARG(v, i) ((gp_t)(((unsigned long long)v[2 * (i) + 1] << 32) | v[2 * (i)]))
  __shared__ __attribute__((aligned(1024))) unsigned char smem[L_END];
  float* const xs = reinterpret_cast<float*>(smem + L_XS);
  unsigned* const xraw = reinterpret_cast<unsigned*>(smem + L_XRAW);
  f32x4* const part = reinterpret_cast<f32x4*>(smem + L_PART);
  unsigned* const qs = reinterpret_cast<unsigned*>(smem + L_QS);
  unsigned* const hs = reinterpret_cast<unsigned*>(smem + L_HS);
  float* const wmax = reinterpret_cast<float*>(smem + L_WMAX);
  float* const lsum = reinterpret_cast<float*>(smem + L_LSUM);
  unsigned* const ctl = reinterpret_cast<unsigned*>(smem + L_CTL);
  const unsigned lb = lds_addr(smem);
  STAMP(0);
  __builtin_amdgcn_s_setprio(3);
  const float L2E = 1.4426950408889634f;
  const int tid = threadIdx.x, lane = tid & 63, r8 = lane >> 3, c8 = lane & 7;
  int w = __builtin_amdgcn_readfirstlane(tid >> 6);
  const int b = blockIdx.x, xcd = b & 7, j = b >> 3, h = xcd + 8 * (j >> 3), s = j & 7;
  const bool lt = xcd & 1;
  const bool one = lt || j < 16;
#define BLK(i) ((h * SK + (SK - WIN) + ((i) * 8 + s) * 32) * (DH * 2))
#define LDK() { const rsrc_t rk = mk((const void*)ARG(ka, 1), H * SK * DH * 2); \
    _Pragma("unroll") for (int g = 0; g < 4; ++g) _Pragma("unroll") for (int jj = 0; jj < 3; ++jj) \
    K[3 * g + jj] = __builtin_amdgcn_raw_buffer_load_b128(rk, vkv + jj * 128, sb + g * 3072, NT); }
#define SHIFT(hd, r, t) (((int)(((hd) * (r) / 32u + (r) - (((t) & 0xFFFFFFFu) * 4u) % (r)) % (r)) - (int)(r) / 2) / 4)
#define PACE(due) { PIN(); const unsigned d_ = (due); while ((int)((unsigned)__builtin_amdgcn_s_memrealtime() - d_) < 0) __builtin_amdgcn_s_sleep(1); PIN(); }
#define DUE(n) (tz + (n) * pd / 4)
#define KPACE(n) { tz = lds_ld(&ctl[30]); tz += SHIFT(h, (unsigned)pd, tz) + PAR(0, P0); PACE(DUE(n)) }
  unsigned ta = 0u, tb = 0u;
  unsigned tz;
  const int pw = (w + 14) & 15, vkv = r8 * 384 + c8 * 16, sb = BLK(pw);
  const bool t1 = (lane >> 3) & 1;
#define PAIRS() const int k0 = 6 * (lane >> 4) + ((lane >> 1) & 4), k2 = (lane & 8) ? k0 : k0 + 2; \
  const int pr0 = 32 * (k0 >> 3) + 4 * c8 + ((k0 & 7) >> 1), pr1 = 32 * (k2 >> 3) + 4 * c8 + ((k2 & 7) >> 1)
  if (tid < 30) ctl[tid] = 0u;
  int sq[3], sg = w < 2 ? 8 * (H * DH * 2) : 3072, vq = r8 * (sg >> 3) + c8 * 16;
#pragma unroll
  for (int jj = 0; jj < 3; ++jj) {
    const int c = 6 * xcd + 3 * w + jj;
    int t = (c & 15) * 40 * (H * DH * 2) + j * (DH * 2) + (c >> 4) * 128;
    asm volatile("" : "+s"(t));
    sq[jj] = w < 2 ? t : sb + jj * 128;
    asm volatile("" : "+s"(sq[jj]));
  }
  asm volatile("" : "+s"(sg));
  int vq4 = w < 2 ? vq : OOB, vx = w < 2 ? lane * 4 : OOB;
  PINV(vq); PINV(vq4); PINV(vx);
  unsigned xr[5];
  u32x4 W[15];
#define K W
  if (w >= 2) __builtin_amdgcn_s_setprio(2);
  PIN();
  ARGS();
  if (w == 15) ctl[30] = (unsigned)__builtin_amdgcn_s_memrealtime();
  asm volatile("s_waitcnt lgkmcnt(0)\n\ts_cmp_lt_u32 %0, 2\n\ts_cbranch_scc1 1f\n\ts_barrier\n\t1:" : "+s"(w) :: "scc", "memory");
  if (w < 3) {
    const rsrc_t rx = mk((const void*)ARG(ka, 0), D * 2), rq = mk((const void*)(w < 2 ? ARG(ka, 2) : ARG(ka, 1)), w < 2 ? D * H * DH * 2 : H * SK * DH * 2);
#pragma unroll
    for (int k = 0; k < 5; ++k) xr[k] = __builtin_amdgcn_raw_buffer_load_b32(rx, vx + k * 256, 0, 0);
    PIN();
#pragma unroll
    for (int g = 0; g < 5; ++g) {
#pragma unroll
      for (int jj = 0; jj < 3; ++jj)
        W[3 * g + jj] = __builtin_amdgcn_raw_buffer_load_b128(rq, g < 4 ? vq : vq4, sq[jj] + g * sg, NT);
      PIN();
    }
  }
  asm volatile("s_cmp_ge_u32 %0, 2\n\ts_cbranch_scc1 1f\n\ts_barrier\n\t1:" : "+s"(w) :: "scc", "memory");
  if (w > 2) {
    RTL();
    if (w >= 14) code_ahead(2 * j + (w & 1));
    const int pd = one ? PAR(1, KD) : PAR(2, KDB);
    KPACE(pw < QM ? pw : w)
    LDK()
    PIN();
  }
  asm volatile("" : "+s"(w));
  if (w < 2) {
    unsigned char* ws = (unsigned char*)ARG(ka, 3);
    const rsrc_t rw = mk(ws, WS_END);
    unsigned long long eps;
    asm volatile("s_load_dwordx2 %0, %1, 0x0" : "=&s"(eps) : "s"(ws) : "memory");
    PIN();
    PAIRS();
    if (w == 1 && lane < 56) hs[40 + lane] = (lane >= 8 && lane < 48) ? 0x3F803F80u : 0u;
    PIN();
    {
      float sm = 0.f;
#pragma unroll
      for (int k = 0; k < 5; ++k) sm += lo16(xr[k]) + hi16(xr[k]);
      const float mean = red64(sm) * (1.f / D);
      float q2 = 0.f;
#pragma unroll
      for (int k = 0; k < 5; ++k) { const float a = lo16(xr[k]) - mean, bb = hi16(xr[k]) - mean; q2 += a * a + bb * bb; }
      const float var = bfr(red64(q2) * (1.f / D));
      const float mb = bfr(mean), den = bfr(sqrtf(bfr(var + 1e-5f)));
#pragma unroll
      for (int k = 0; k < 5; ++k) {
        f32x2 v = {bfr(bfr(lo16(xr[k]) - mb) / den), bfr(bfr(hi16(xr[k]) - mb) / den)};
        *reinterpret_cast<f32x2*>(&xs[2 * (lane + 64 * k)]) = v;
        xraw[lane + 64 * k] = xr[k];
      }
    }
    __builtin_amdgcn_fence(__ATOMIC_RELEASE, "wavefront");
    __builtin_amdgcn_wave_barrier();
    STAMP(12);
    {
      unsigned x01[3], x23[3], x4[3];
#pragma unroll
      for (int jj = 0; jj < 3; ++jj) {
        const float* xp = &xs[((6 * xcd + 3 * w + jj) & 15) * 40 + r8];
        x01[jj] = pk2(xp[0], xp[8]); x23[jj] = pk2(xp[16], xp[24]); x4[jj] = pk2(xp[32], 0.f);
      }
      float a[24];
#pragma unroll
      for (int k = 0; k < 24; ++k) a[k] = 0.f;
      pv_pair(a, &W[0], &W[3], x01);
      pv_pair(a, &W[6], &W[9], x23);
      pv_pair(a, &W[12], &W[12], x4);
      const f32x4 r4 = red_rows(a, t1);
      f32x2* const pp2 = reinterpret_cast<f32x2*>(part + 48 * w);
      if (!t1) pp2[pr1] = f32x2{r4.z, r4.w};
      pp2[pr0] = f32x2{r4.x, r4.y};
    }
    lds_inc(&ctl[2], lane);
    asm volatile("s_waitcnt lgkmcnt(0)" : "+s"(eps) :: "memory");
    const unsigned long long en = eps + 1ull;
    ta = (unsigned)en; tb = (unsigned)(en >> 32) ^ 0x9E3779B9u;
    if (lane == 0) { ctl[0] = ta; ctl[1] = tb; }
    if (!w) {
      while (lds_ld(&ctl[2]) != 2u) {}
      asm volatile("" ::: "memory");
      const int cl = (6 * xcd >> 4) + (lane >> 5);
      f32x2 a2 = {0.f, 0.f};
#pragma unroll
      for (int i = 0; i < 6; ++i) { const f32x2 t = reinterpret_cast<const f32x2*>(part)[32 * i + (lane & 31)]; if (((6 * xcd + i) >> 4) == cl) a2 += t; }
      put_unit<SC1 | NT>(rw, cl <= ((6 * xcd + 5) >> 4) ? WS_PQ + (j * 10 + xcd + (xcd > 2) + (xcd > 5)) * 512 + lane * 16 : OOB, a2.x, a2.y, ta, tb);
      if (j == 0) put_unit(rw, lane < 8 ? WS_TO + xcd * 128 + lane * 16 : OOB, __uint_as_float(lds_ld(&ctl[30])), 0.f, ta, tb);
    }
    __builtin_amdgcn_s_setprio(2);
    RTL();
    const int pd = one ? PAR(1, KD) : PAR(2, KDB);
    KPACE(QM + w)
    LDK()
    PIN();
  }
#define F_(x) __uint_as_float(x)
  asm volatile("s_load_dwordx8 %0, %2, 0x20\n\ts_load_dwordx4 %1, %2, 0x40\n\ts_waitcnt lgkmcnt(0)" : "=&s"(kb), "=&s"(kc4) : "s"(kap) : "memory");
  RTL();
  asm volatile("" : "+s"(w));
  unsigned char* ws = (unsigned char*)ARG(ka, 3);
  const u16* wo = (const u16*)ARG(kb, 1); const u16* w1 = (const u16*)ARG(kb, 2); const u16* w3 = (const u16*)ARG(kb, 3);
  const u16* w2 = (const u16*)ARG(kc4, 0); u16* out = (u16*)ARG(kc4, 1);
  const rsrc_t rl = mk(ws, WS_END);
  const int pd = one ? PAR(1, KD) : PAR(2, KDB);
  const int dw = w & 1, csw = (c8 ^ (r8 & 2) ^ (4 * dw)) * 16;
  {
    lds_inc(&ctl[4], lane);
    if (w == 0) {
      __builtin_amdgcn_s_setprio(3);
      u32x4 qv[5], ov[1];
      int qo = WS_PQ + h * 5120 + lane * 16, oo = WS_TO + c8 * 128;
#pragma unroll 1
      for (unsigned spins = 0; spins < SPIN_CAP; ++spins) {
        asm volatile("" : "+v"(qo), "+v"(oo) :: "memory");
        load_units<5, 1024, SC1 | NT>(rl, qo, qv);
        load_units<1, 0>(rl, oo, ov);
        if (none(units_bad<1>(ov, ta, tb, units_bad<5>(qv, ta, tb)) != 0u)) break;
      }
      f32x2 v[5];
#pragma unroll
      for (int k = 0; k < 5; ++k) { const unsigned u0 = qv[k].x, u1 = qv[k].z; v[k] = f32x2{__uint_as_float(u0), __uint_as_float(u1)}; }
      const bool hi = lane >= 32;
      const f32x2 c0 = hi ? v[0] : v[0] + v[1], c1 = hi ? v[1] + v[2] : v[2] + v[3], c2 = hi ? v[3] + v[4] : v[4];
      qs[lane] = pk2(sw32(c0.x, c1.x), sw32(c0.y, c1.y));
      qs[64 + (lane & 31)] = pk2(sw32(c2.x, c2.x), sw32(c2.y, c2.y));
      const unsigned o_ = ov[0].x, o0 = __builtin_amdgcn_readfirstlane(o_);
      float od = (float)(int)(o_ - o0);
      od += dppf<XOR1>(od); od += dppf<XOR2>(od); od += dppf<HMIR>(od);
      asm volatile("s_waitcnt lgkmcnt(0)" ::: "memory");
      ctl[3] = (o0 + (unsigned)(int)(od * 0.125f)) | 1u;
    }
  }
  __builtin_amdgcn_s_setprio(1);
  __builtin_amdgcn_s_waitcnt(0x0F70);
  asm volatile("" :: "v"(W[12]), "v"(W[13]), "v"(W[14]), "v"(xr[0]), "v"(xr[1]), "v"(xr[2]), "v"(xr[3]), "v"(xr[4]));
  unsigned tc;
  float wm;
  {
    const rsrc_t rv = mk((const void*)ARG(kb, 0), H * SK * DH * 2);
    const int vv = r8 * 384 + csw;
#define LDV(vg) { WAITC(4, NST + 2 * (vg)); \
    int so_ = BLK(4 * (vg)) + dw * 3072; unsigned ld_ = lb + L_RING + ((vg) == 3 ? 0 : (vg)) * 49152 + dw * 3072; \
    _Pragma("unroll 1") for (int i = 4 * (vg); i < 4 * (vg) + 4; ++i) { PACE(DUE(i)) \
      dma3(rv, vv, so_, ld_); dma3(rv, vv, so_ + 6144, ld_ + 6144); so_ += 98304; ld_ += 12288; } \
      lds_inc(&ctl[4], lane); }
    unsigned tg;
    while ((tg = lds_ld(&ctl[3])) == 0u) __builtin_amdgcn_s_sleep(2);
    asm volatile("" ::: "memory");
    tz = tg + SHIFT(h, (unsigned)pd, tg) + PAR(0, P0) + GK + 4 * pd;
    tc = tg + PAR(0, P0) + GK + 31 * PAR(1, KD) / 4;
    ta = __builtin_amdgcn_readfirstlane(ctl[0]); tb = __builtin_amdgcn_readfirstlane(ctl[1]);
    float pe;
    {
      const unsigned* qp = &qs[4 * c8];
      u32x4 qf[3];
#pragma unroll
      for (int jj = 0; jj < 3; ++jj) qf[jj] = *reinterpret_cast<const u32x4*>(qp + 32 * jj);
      float d[4];
#pragma unroll
      for (int g = 0; g < 4; ++g) {
        d[g] = 0.f;
#pragma unroll
        for (int jj = 0; jj < 3; ++jj)
#pragma unroll
          for (int i = 0; i < 4; ++i) d[g] = dot2(K[3 * g + jj][i], qf[jj][i], d[g]);
        d[g] += dppf<HMIR>(d[g]);
      }
      const float e0 = rs<XOR2>(d[0], d[2], lane & 2), e1 = rs<XOR2>(d[1], d[3], lane & 2);
      float sc = rs<XOR1>(e0, e1, lane & 1);
      sc = bfr(bfr(sc) * 0.07216878364870322f);
      wm = max64(sc);
      pe = sc;
      if (lane == 0) wmax[w] = wm;
      lds_inc(&ctl[9], lane);
    }
    if ((w >> 1) == 1) LDV(0)
    while (lds_ld(&ctl[9]) < NST) __builtin_amdgcn_s_sleep(2);
    asm volatile("" ::: "memory");
    u16* const pp = reinterpret_cast<u16*>(smem + L_P) + 32 * pw + 8 * (lane & 3) + r8;
    wm = max16(wmax[lane & 15]);
    pe = __builtin_amdgcn_exp2f((pe - wm) * L2E);
    {
      const float p = bfr(pe);
      *pp = (u16)(__float_as_uint(p) >> 16);
      const float ls = red64(p) * 0.5f;
      if (lane == 0) lsum[w] = ls;
    }
    lds_inc(&ctl[10], lane);
    if (w >= 2) lds_inc(&ctl[16 + (pw >> 2)], lane);
    const int vg = (0x42144403 >> (4 * (w >> 1))) & 15;
    if (vg == 3) {
      WAITC(10, NST);
      *pp = (u16)pk2(pe * __builtin_amdgcn_rcpf(red16(lsum[lane & 15])), 0.f);
      WAITC(20, 6u);
    }
    if (vg < 4) {
      if (vg) LDV(vg)
      if (vg == 3) VMW(12); else VMW(0);
      lds_inc(&ctl[16 + vg], lane);
    }
  }
  const bool role4 = one, pvw = w >= 4 && w < 10;
  const int q3 = j & 3;
  const int ho = ((!role4 && q3 == 3) ? xcd : xcd ^ 1) + 8 * (lt ? j >> 3 : (j >> 2) & 3), ob = lt ? j & 7 : role4 ? q3 : (q3 == 3 ? 8 : 4 + 2 * q3);
  const int rg = j < 24 ? xcd : xcd ^ 1, ln = j < 24 ? j : j - 8, n5 = 12 * (xcd >> 1) + j - 16;
  const int g4 = lane >> 4, tq = (lane >> 2) & 3, tp = lane & 3;
#define FRAG(t) (g4 * 1024 + tq * 128 + (((2 * (t) + (tp >> 1)) ^ (tq & 2) ^ (4 * (g4 & 1))) * 16) + (tp & 1) * 8)
  const int vo = r8 * (D * 2) + csw;
  const unsigned n12 = (role4 ? 4u : 8u);
  if (pvw) {
    const int t2 = w - 4;
    unsigned va = lb + L_RING + 2 * g4 * 1024 + (t2 >> 1) * 1024 + FRAG(2 * (t2 & 1)), vb = va ^ 32u, pa = lb + L_P + 16 * g4;
    f32x4 ac0 = {0.f, 0.f, 0.f, 0.f}, ac1 = {0.f, 0.f, 0.f, 0.f};
#define PVB(i) { const u32x4 pf = lds_ld4(pa, 64 * (i)); \
    const u32x4 a0 = frag(va, ((i) & 3) * 12288, ((i) & 3) * 12288 + 512), a1 = frag(vb, ((i) & 3) * 12288, ((i) & 3) * 12288 + 512); \
    ac0 = mma(a0, pf, ac0); ac1 = mma(a1, pf, ac1); }
#define PVD() PINV(ac0); PINV(ac1); PIN();
    WAITC(16, 6u);
    PVB(0) PVB(1) PVB(2) PVB(3) va += 49152; vb += 49152;
    lds_inc(&ctl[20], lane);
    PVD()
    WAITC(17, 6u);
    PVB(4) PVB(5) PVB(6) PVB(7) va += 49152; vb += 49152;
    lds_inc(&ctl[21], lane);
    PVD()
    WAITC(10, NST);
    float rl_ = __builtin_amdgcn_rcpf(red16(lsum[lane & 15]));
    PINV(rl_);
    WAITC(18, 6u);
    PVB(8) PVB(9) PVB(10) PVB(11) va -= 98304; vb -= 98304;
    lds_inc(&ctl[22], lane);
    PVD()
    __builtin_amdgcn_s_setprio(3);
    SPINC(19, 4u);
    PVB(12) PVB(13)
    ac0 *= rl_; ac1 *= rl_;
    u32x4 pf = lds_ld4(pa, 64 * 14), pg = lds_ld4(pa, 64 * 15);
    int p1off = (lane & 14) ? OOB : WS_P1 + (NREC * h + s) * 1024 + (8 * t2 + 4 * (lane & 1) + g4) * 16;
    PINV(ac0); PINV(ac1); PINV(pf); PINV(pg); PINV(p1off); PINV(va); PINV(vb);
    SPINC(23, 2u);
    const u32x4 a0 = frag(va, 2 * 12288, 2 * 12288 + 512), a1 = frag(vb, 2 * 12288, 2 * 12288 + 512);
    const u32x4 a2 = frag(va, 3 * 12288, 3 * 12288 + 512), a3 = frag(vb, 3 * 12288, 3 * 12288 + 512);
    PIN();
    ac0 = mma(a0, pf, ac0); ac1 = mma(a1, pf, ac1);
    ac0 = mma(a2, pg, ac0); ac1 = mma(a3, pg, ac1);
    const unsigned x0 = pk2(ac0.x, ac0.y), z0 = pk2(ac0.z, ac0.w), x1 = pk2(ac1.x, ac1.y), z1 = pk2(ac1.z, ac1.w);
    put_unit(rl, p1off, __uint_as_float((lane & 1) ? x1 : x0), __uint_as_float((lane & 1) ? z1 : z0), ta, tb);
    STAMP(4);
  } else if (w < 2) {
    __builtin_amdgcn_s_setprio(3);
    VMW(0);
    lds_inc(&ctl[23], lane);
    const int lag = (int)((unsigned)__builtin_amdgcn_s_memrealtime() - tc) - (PAR(6, TY) & 255);
    if (!w) ctl[26] = ((tc + (lag > 0 ? lag : 0)) | 1u);
    BAR();
  } else if (w < 4) {
    const rsrc_t ro = mk(wo, H * DH * D * 2);
    const int so_o = ((ho * DH + 8 * dw) * D + 64 * ob) * 2;
    const unsigned t0 = role4 ? tc + (PAR(7, TW) & 255) : DUE(16), dt = role4 ? WQD : pd / 8, dg = role4 ? PAR(7, TW) >> 8 : 0;
    WAITS(21, 6u);
    WAITS(4, (NST + 8));
    __builtin_amdgcn_s_setprio(3);
#pragma unroll 1
    for (int q = 0; q < (role4 ? 4 : 8); ++q) {
      until((t0 + q * dt + (q ? dg : 0)));
#pragma unroll
      for (int n = 0; n < 3; ++n)
        dma1(ro, vo, so_o + (q >> 2) * 128 + ((q & 3) * 3 + n) * 16 * D * 2, lb + L_WO + (2 * (3 * q + n) + dw) * 1024);
    }
    VMW(3);
    lds_inc(&ctl[28], lane);
    VMW(0);
    BAR0();
  } else if (w == 11) {
    WAITS(10, NST);
    const float lse = fmaf(wm, L2E, __builtin_amdgcn_logf(red16(lsum[lane & 15])));
    PACE(tc + TP)
    put_unit(rl, lane < 8 ? WS_P1 + (NREC * h + s) * 1024 + 768 + lane * 16 : OOB, lse, lse, ta, tb);
    PACE(tc + TL)
    u32x4 lv_[1];
    poll_units<1, 0>(rl, WS_P1 + (ho * NREC + c8) * 1024 + 768, lv_, ta, tb);
    const unsigned mu = lv_[0].x;
    const float ms = __uint_as_float(mu);
    float Mg = fmaxf(ms, dppf<XOR1>(ms)); Mg = fmaxf(Mg, dppf<XOR2>(Mg)); Mg = fmaxf(Mg, dppf<HMIR>(Mg));
    const float wg = __builtin_amdgcn_exp2f(ms - Mg);
    float Lg = wg + dppf<XOR1>(wg); Lg += dppf<XOR2>(Lg); Lg += dppf<HMIR>(Lg);
    lds_st(lb + L_WRE + 4 * c8, __float_as_uint(wg * __builtin_amdgcn_rcpf(Lg)));
    lds_inc(&ctl[28], lane);
  }
  __builtin_amdgcn_s_setprio(3);
  if (w >= (role4 ? 12 : 8)) {
    const int bk = w < 12;
    unsigned ba = lb + L_WO + bk * 24576 + FRAG(w & 3);
    unsigned ao = lb + L_REC + c8 * 416 + 16 * g4;
    int so = lane < 8 ? WS_P2 + (ho * 320 + (ob + bk) * 32 + 8 * (w & 3) + lane) * 16 : OOB;
    int ro_ = lane < 48 ? WS_P1 + (ho * NREC + 2 * (w & 3)) * 1024 + lane * 16 : OOB;
    unsigned ra = lb + L_REC + (w & 3) * 832 + (lane < 48 ? lane * 8 : 384);
    unsigned tl = lane < 48 ? ta : 0u, th = lane < 48 ? tb : 0u;
    PINV(ba); PINV(ao); PINV(so); PINV(ro_); PINV(ra); PINV(tl); PINV(th);
    u32x4 bf[6], af[6], v[2], wu;
    if (!bk) {
      WAITS(26, 1u);
      until(lds_ld(&ctl[26]) + PAR(3, TB));
      asm volatile("buffer_load_dwordx4 %0, %2, %3, 0 offen sc1\n\tbuffer_load_dwordx4 %1, %2, %3, 0 offen offset:1024 sc1" : "=&v"(v[0]), "=&v"(v[1]) : "v"(ro_), "s"(rl) : "memory");
      STAMP(15);
#define AHEAD() SPINC(28, 3u);                 \
      _Pragma("unroll") for (int ks = 0; ks < 4; ++ks) bf[ks] = frag(ba, ks * 4096, ks * 4096 + 512); \
      wu = lds_ld4(lb + L_WRE + 16 * (g4 & 1), 0); \
      _Pragma("unroll") for (int ks = 0; ks < 4; ++ks) PINV(bf[ks]); \
      PINV(wu); PIN();
      AHEAD()
      asm volatile("s_waitcnt vmcnt(0)" : "+v"(v[0]), "+v"(v[1]) :: "memory");
#define RECS() _Pragma("unroll") for (int k = 0; k < 2; ++k) *(__attribute__((address_space(3))) u32x2*)(size_t)(ra + k * 416) = u32x2{v[k].x, v[k].z};
      RECS()
      if (RARE(!none(units_bad<2>(v, tl, th) != 0u))) {
        CNT((poll_lane<2, 1024>(rl, ro_, v, tl, th)));
        RECS()
      }
    } else {
      WAITS(28, 1u);
      AHEAD()
    }
    BAR();
    const f32x4 wv = __builtin_bit_cast(f32x4, wu);
#pragma unroll
    for (int ks = 0; ks < 4; ++ks) af[ks] = lds_ld4(ao, 64 * ks);
#pragma unroll
    for (int ks = 4; ks < 6; ++ks) { af[ks] = lds_ld4(ao, 64 * ks); bf[ks] = frag(ba, ks * 4096, ks * 4096 + 512); }
    PIN();
    f32x4 acc = {0.f, 0.f, 0.f, 0.f}, ac2 = {0.f, 0.f, 0.f, 0.f};
#pragma unroll
    for (int ks = 0; ks < 6; ks += 2) { acc = mma(af[ks], bf[ks], acc); ac2 = mma(af[ks + 1], bf[ks + 1], ac2); }
    float c0 = fmaf(wv.w, acc.w, fmaf(wv.z, acc.z, fmaf(wv.y, acc.y, wv.x * acc.x)));
    const float c2 = fmaf(wv.y, ac2.y, wv.x * ac2.x), c3 = fmaf(wv.w, ac2.w, fmaf(wv.z, ac2.z, c0));
    c0 = c2 + c3;
    c0 = sw16(c0, c0);
    const float c1 = dppf<ROR8>(c0);
    put_unit(rl, so, c0, c1, ta, tb);
    PIN();
    lds_inc(&ctl[12], lane);
    STAMP(6);
  }
  if (role4) {
    if ((0x0E0C >> w) & 1) {
      if (w >= 4) BAR0();
      const int hw = w < 4 ? w - 2 : w - 7;
      const int cl = 16 * hw + c8 + 8 * (lane >> 5);
      float xv = lo16(reinterpret_cast<const u16*>(xraw)[80 * rg + cl]);
      unsigned hoa = lb + L_HS + 2 * cl;
      u32x4 v[4];
      int po = WS_P2 + (r8 * 320 + 40 * rg + 8 * hw + c8) * 16;
      PINV(xv); PINV(hoa); PINV(po);
      const unsigned th_ = (lds_ld(&ctl[26]) + PAR(5, TD));
      __builtin_amdgcn_s_sleep(HS);
      until(th_);
      load_units<4, 8 * 320 * 16>(rl, po, v);
      CNT((poll_lane<4, 8 * 320 * 16>(rl, po, v, ta, tb)));
      float s0 = (F_(v[0].x) + F_(v[1].x)) + (F_(v[2].x) + F_(v[3].x));
      const float s1 = (F_(v[0].z) + F_(v[1].z)) + (F_(v[2].z) + F_(v[3].z));
      s0 = sw32(s0, s1);
      s0 = sw16(s0, s0);
      s0 += dppf<ROR8>(s0);
      s0 = bfr(s0) + xv;
      *(__attribute__((address_space(3))) u16*)(size_t)hoa = (u16)pk2(s0, s0);
      BAR();
      STAMP(7);
    } else if (w < 13) {
      if (w >= 4 && w < 9) {
        const rsrc_t r1 = mk(w1, D * F * 2), r3 = mk(w3, D * F * 2);
        const int q = w - 4;
        const int v0 = r8 * (F * 2) + csw, v1 = v0 ^ 64, so = ((80 * rg + 8 * q) * F + ln * 64) * 2;
        const unsigned fd = lb + L_FF + q * 1024;
        WAITC(22, 6u);
        WAITS(26, 1u);
        until((lds_ld(&ctl[26]) + PAR(3, TB) + F1D + (q > 1 ? F1S : 0)));
        dma1(r1, v0, so, fd); dma1(r3, v0, so, fd + 10240);
        dma1(r1, v1, so + 40 * F * 2, fd + 5120); dma1(r3, v1, so + 40 * F * 2, fd + 15360);
        BAR0();
        VMW(0);
        lds_inc(&ctl[13], lane);
      } else if (w < 2) {
        WAITC(12, n12);
        if (PAR(4, FL) & 16) PACE(lds_ld(&ctl[26]) + PAR(5, TD) + PW)
        if (!(PAR(4, FL) & 32)) put_unit(rl, WS_G + (((2 * ln + w) * 8 + rg) * 64 + lane) * 16, 0.f, 0.f, ~ta, ~tb);
      }
      const int tw = w < 2 ? w : w < 9 ? w - 2 : 7;
      unsigned bo = lb + L_FF + (tw >> 2) * 10240 + FRAG(tw & 3), b2 = bo + (g4 < 2 ? 8192 : 0);
      int go4 = lane < 16 ? WS_G + (((2 * ln + ((tw >> 1) & 1)) * 8 + rg) * 64 + (tw >> 2) * 32 + (tw & 1) * 16 + lane) * 16 : OOB;
      unsigned ao = lb + L_HS + 192 * (lane & 1) + 16 * g4;
      PINV(go4); PINV(ao); PINV(bo); PINV(b2);
      u32x4 af[3], bf[3];
      WAITC(13, 5u);
      bf[0] = frag(bo, 0, 512); bf[1] = frag(bo, 4096, 4096 + 512); bf[2] = frag(b2, 0, 512);
      PINV(bf[0]); PINV(bf[1]); PINV(bf[2]);
      VMW(0);
      BAR0();
#pragma unroll
      for (int ks = 0; ks < 3; ++ks) af[ks] = lds_ld4(ao, 64 * ks);
      PIN();
      f32x4 acc = {0.f, 0.f, 0.f, 0.f};
#pragma unroll
      for (int ks = 0; ks < 3; ++ks) acc = mma(af[ks], bf[ks], acc);
      put_unit(rl, go4, acc.x, acc.y, ta, tb);
      STAMP(8);
    } else if (w == 13) {
      unsigned char* go = ws + slot(G3 + 2 * ln + (lane & 1), rg);
      unsigned mt = ta;
      PINV(go); PINV(mt);
      BAR0();
      RING(go, mt, 3ull);
      __builtin_amdgcn_raw_buffer_store_b64(u32x2{ta, tb ^ 0x9E3779B9u}, rl, (b == 0 && lane == 0) ? 0 : OOB, 0, SC1);
      STAMP(12);
    } else if (w == 14) {
      unsigned hi_ = lb + L_HS + 4 * (lane < 40 ? lane : 0);
      int sto = lane < 8 ? WS_ST + (ln * 8 + rg) * 128 + lane * 16 : OOB;
      PINV(hi_); PINV(sto);
      if (PAR(4, FL) & 16) PACE(lds_ld(&ctl[26]) + PAR(5, TD) + PW)
      if (!(PAR(4, FL) & 32)) put_unit(rl, sto, 0.f, 0.f, ~ta, ~tb);
      VMW(0);
      BAR0();
      const unsigned hp = *(lds_u32)(size_t)hi_;
      const float h0 = lane < 40 ? lo16(hp) : 0.f, h1 = lane < 40 ? hi16(hp) : 0.f;
      const float cs = __uint_as_float(__builtin_amdgcn_readfirstlane(hp) << 16);
      const float d0 = lane < 40 ? h0 - cs : 0.f, d1 = lane < 40 ? h1 - cs : 0.f;
      float sr = red32(sw32(d0 + d1, d0 * d0 + d1 * d1));
      const float s2 = __uint_as_float(__builtin_amdgcn_readlane(__float_as_uint(sr), 32));
      const float mw = fmaf(sr, 1.f / 80.f, cs), m2 = fmaxf(fmaf(-sr * (1.f / 80.f), sr, s2), 0.f);
      put_unit(rl, sto, mw, m2, ta, tb);
    } else if (j < 5) {
      const rsrc_t rout = mk(out, (D * 2));
      const int cl = 16 * j + c8 + 8 * (lane >> 5);
      unsigned char* gi = ws + slot(G4 + 3 * (b % NG4), lane < N5 ? lane : 0);
      int op = (lane & 24) ? OOB : (80 * xcd + cl) * 2;
      int po = WS_P5 + (r8 * 320 + 40 * xcd + 8 * j + c8) * 16;
      unsigned sl[4];
      PINV(gi); PINV(op); PINV(po);
      BAR0();
      float hv = lo16(reinterpret_cast<const u16*>(hs)[cl]);
      PINV(hv);
      __builtin_amdgcn_s_sleep(FSL);
      until(gate(gi, ta, (1ull << N5) - 1ull, sl) + D4);
      u32x4 v[6];
      load_units<6, 8 * 320 * 16>(rl, po, v);
      PIN();
#define FIN_() { float s0 = F_(v[0].x), s1 = F_(v[0].z); bad = 0u; \
        _Pragma("unroll") for (int k = 0; k < 6; ++k) { if (k) { s0 += F_(v[k].x); s1 += F_(v[k].z); } \
          if (k < 5) { bad = __builtin_amdgcn_bitop3_b32(v[k].y, ta, bad, 0xBE); bad = __builtin_amdgcn_bitop3_b32(v[k].w, tb, bad, 0xBE); PINV(s0); PINV(s1); PINV(bad); PIN(); } } \
        s0 = sw32(s0, s1); \
        bad = __builtin_amdgcn_bitop3_b32(v[5].y, ta, bad, 0xBE); bad = __builtin_amdgcn_bitop3_b32(v[5].w, tb, bad, 0xBE); \
        s0 = sw16(s0, s0); s0 += dppf<ROR8>(s0); s0 = bfr(s0) + hv; res = pk2(s0, s0); }
      unsigned res, bad;
      FIN_()
      PINV(res);
      if (RARE(!none(bad != 0u))) {
        CNT((poll_units<6, 8 * 320 * 16>(rl, po, v, ta, tb)));
        FIN_()
      }
      __builtin_amdgcn_raw_buffer_store_b16((u16)res, rout, op, 0, 0);
      STAMP(17);
      KEEP(sl);
      asm volatile("s_endpgm");
    }
  } else if (j < 28) {
    if (w == 3) {
      unsigned char* gi = ws + slot(G3 + n5, lane & 7);
      unsigned ai = lb + L_ACT + (lane < 32 ? 2 * lane : 64), fa = lb + L_CTL + 24 * 4;
      int go_ = WS_G + (n5 * 8 * 64 + lane) * 16, so_ = WS_ST + ((n5 >> 1) * 8 + c8) * 128;
      unsigned sl[4], lvv = 1u;
      PINV(gi); PINV(ai); PINV(fa); PINV(go_); PINV(so_); PINV(lvv);
      WAITC(12, n12);
      if (PAR(4, FL) & 8) PACE(lds_ld(&ctl[26]) + PAR(5, TD) + MS)
      wait_marks(gi, ta, 0xFFull, sl);
      u32x4 gv[8], sv[1];
      load_units<1, 0>(rl, so_, sv);
      load_units<8, 1024>(rl, go_, gv);
      PIN();
      lds_st(fa, lvv);
      PIN();
#define ACT_() { const float c0s = F_(__builtin_amdgcn_readfirstlane(sv[0].x));       \
        float da = F_(sv[0].x) - c0s, m2 = fmaf(80.f * da, da, F_(sv[0].z)); \
        da += dppf<XOR1>(da); m2 += dppf<XOR1>(m2); da += dppf<XOR2>(da); m2 += dppf<XOR2>(m2); da += dppf<HMIR>(da); m2 += dppf<HMIR>(m2); \
        da *= 0.125f; \
        const float var = bfr(fmaxf(fmaf(-da, da, m2 * (1.f / D)), 0.f)); \
        float mb = bfr(c0s + da), rd = __builtin_amdgcn_rcpf(bfr(__builtin_amdgcn_sqrtf(bfr(var + 1e-5f)))); \
        PINV(mb); PINV(rd); PIN(); \
        float gs_ = F_(gv[0].x), cs_ = F_(gv[0].z); \
        bad = __builtin_amdgcn_bitop3_b32(sv[0].y, ta, 0u, 0xBE); bad = __builtin_amdgcn_bitop3_b32(sv[0].w, tb, bad, 0xBE); \
        _Pragma("unroll") for (int k = 0; k < 8; ++k) { if (k) { gs_ += F_(gv[k].x); cs_ += F_(gv[k].z); } \
          if (k < 7) { bad = __builtin_amdgcn_bitop3_b32(gv[k].y, ta, bad, 0xBE); bad = __builtin_amdgcn_bitop3_b32(gv[k].w, tb, bad, 0xBE); PINV(gs_); PINV(cs_); PINV(bad); PIN(); } } \
        float val = bfr((gs_ - mb * cs_) * rd); \
        float sg = bfr(val * __builtin_amdgcn_rcpf(1.f + __builtin_amdgcn_exp2f(-val * L2E))); \
        const float av = sg * F_(__builtin_amdgcn_permlane32_swap(__float_as_uint(val), __float_as_uint(val), false, false)[1]);     \
        *(__attribute__((address_space(3))) u16*)(size_t)ai = (u16)pk2(av, av); \
        bad = __builtin_amdgcn_bitop3_b32(gv[7].y, ta, bad, 0xBE); bad = __builtin_amdgcn_bitop3_b32(gv[7].w, tb, bad, 0xBE); }
      unsigned bad;
      ACT_()
      if (RARE(!none(bad != 0u))) {
        CNT((poll_units<8, 1024>(rl, go_, gv, ta, tb))); poll_units<1, 0>(rl, so_, sv, ta, tb);
        ACT_()
      }
      BAR();
      KEEP(sl);
    } else if (w == 2) {
      unsigned char* go = ws + slot(G4 + 3 * (lane < NG4 ? lane : 0), n5);
      PINV(go);
      WAITC(13, 7u);
      SPINC(24, 1u);
      announce(go, ta, (unsigned)__builtin_amdgcn_s_memrealtime(), ((1ull << NG4) - 1ull));
      BAR0();
    } else if (w >= 4 && w < 14) {
      if (w < 8) {
        const rsrc_t r2 = mk(w2, F * D * 2);
        const int q = w - 4, so = (R5 * n5 + 8 * q) * D * 2;
        const unsigned fd = lb + L_FF + q * 1024;
        if (W2E) {
          WAITC(22, 6u);
          WAITS(26, 1u);
          until((lds_ld(&ctl[26]) + PAR(3, TB) + W2T));
#pragma unroll
          for (int i = 0; i < W2E; ++i) dma1(r2, vo, so + i * 128, fd + i * 4096);
        }
        BAR0();
        WAITC(12, n12);
#pragma unroll
        for (int i = W2E; i < 10; ++i) dma1(r2, vo, so + i * 128, fd + i * 4096);
        VMW(0);
        lds_inc(&ctl[13], lane);
      }
      const int fw = w - 4;
      int so = (lane & 8) ? OOB : WS_P5 + (n5 * 320 + fw * 32 + 8 * g4 + c8) * 16;
      WAITC(13, 7u);
      u32x4 bf[4];
#pragma unroll
      for (int t = 0; t < 4; ++t) bf[t] = frag(lb + L_FF + fw * 4096 + FRAG(t), 0, 512);
#pragma unroll
      for (int t = 0; t < 4; ++t) asm volatile("" : "+v"(bf[t]));
      unsigned ao = lb + L_ACT + 16 * g4;
      PINV(ao); PINV(so);
      BAR0();
      const u32x4 av = lds_ld4(ao, 0);
      const f32x4 z = {0.f, 0.f, 0.f, 0.f};
      f32x4 d0 = mma(av, bf[0], z), d1 = mma(av, bf[1], z), d2 = mma(av, bf[2], z), d3 = mma(av, bf[3], z);
      asm volatile("" : "+v"(d0), "+v"(d1), "+v"(d2), "+v"(d3));
      float c0 = __uint_as_float(__builtin_amdgcn_update_dpp(__float_as_uint(d0.x), __float_as_uint(d1.x), 0xE4, 0x2, 0xf, false));
      c0 = __uint_as_float(__builtin_amdgcn_update_dpp(__float_as_uint(c0), __float_as_uint(d2.x), 0xE4, 0x4, 0xf, false));
      c0 = __uint_as_float(__builtin_amdgcn_update_dpp(__float_as_uint(c0), __float_as_uint(d3.x), 0xE4, 0x8, 0xf, false));
      const float c1 = dppf<ROR8>(c0);
      put_unit(rl, so, c0, c1, ta, tb);
      PIN();
      STAMP(10);
    } else {
      if (w != 15) {
        WAITC(12, n12);
        if (PAR(4, FL) & 16) PACE(lds_ld(&ctl[26]) + PAR(5, TD) + PW)
        const int pz = (PAR(4, FL) & 64) ? OOB : WS_P5 + (n5 * 320 + (w < 2 ? 128 * w : 256) + lane) * 16;
        put_unit(rl, pz, 0.f, 0.f, ~ta, ~tb);
        put_unit(rl, w < 2 ? pz + 1024 : OOB, 0.f, 0.f, ~ta, ~tb);
        VMW(0);
        lds_inc(&ctl[13], lane);
      }
    }
  }
}
}

extern "C" void kernel_launch(void* const* d_in, const int* in_sizes, int n_in, void* d_out, int out_size, void* d_ws, size_t ws_size,
                              hipStream_t stream) {
  hipLaunchKernelGGL(k_fused, dim3(NWG), dim3(NTHR), 0, stream, (const u16*)d_in[0], (const u16*)d_in[1], (const u16*)d_in[3], (unsigned char*)d_ws,
                     (const u16*)d_in[2], (const u16*)d_in[4], (const u16*)d_in[6], (const u16*)d_in[7], (const u16*)d_in[8], (u16*)d_out
);
}
```

```cpp
#define W2E 6
#define P0 155028073
#define KD 42916
#define KDB 37796
#define TB 15003
#define FL 659218
#define TD 35745032
#define TY 538838377
#define TW 72
#include <hip/hip_runtime.h>
#include <cstdint>

namespace {
constexpr int D = 640, H = 32, DH = 192, SK = 32768, WIN = 4096, F = 1536;
constexpr int NWG = 256, NTHR = 1024;
constexpr int N5 = 48, R5 = F / N5;
constexpr int NST = 16, NREC = 8;
#define QS 5120
constexpr int WS_PQ = QS == 5120 ? 256 : 8 << 20;
constexpr int WS_TO = 256 + 320 * 32 * 16;
constexpr int WS_LB = WS_TO + 1024;
constexpr int WS_P1 = 11 * 16384;
constexpr int WS_P2 = WS_P1 + 16 * 16384;
constexpr int WS_G = WS_P2 + 16 * 16384;
constexpr int WS_ST = WS_G + 24 * 16384;
constexpr int WS_P5 = WS_ST + 2 * 16384;
constexpr int NG4 = 10;
constexpr int WS_GT = WS_P5 + 24 * 16384;
constexpr int WS_END = WS_GT + 2 * 16384;
static_assert(WS_LB + 12288 <= WS_P1, "ws");
constexpr int at_rec(int hd) { return WS_P1 + ((hd & 3) + 4 * (hd >> 3)) * 16384 + (hd & 4) * 2048; }
constexpr int at_row(int n, int tile) { return (n >> 1) * 16384 + (n & 1) * 4096 + (n & 6) * 256 + (tile < 20 ? tile * 128 : 8192 + (tile - 20) * 128); }
constexpr int at_g(int n) { return WS_G + (n < 24 ? n * 16384 : 8192 + (n - 24) * 16384); }
constexpr int at_st(int l) { return WS_ST + (l < 12 ? 0 : 8192) + ((l < 12 ? l : l - 12) >> 3) * 16384 + ((l < 12 ? l : l - 12) & 7) * 1024; }
constexpr int at_m3(int n, int i) { return WS_GT + (n < 24 ? n * 256 : 8192 + (n - 24) * 256) + i * 4; }
constexpr int at_m4(int c, int n) { return WS_GT + 16384 + (c < 5 ? c * 768 : 8192 + (c - 5) * 768) + (n >> 4) * 256 + (n & 15) * 4; }
constexpr int L_RING = 0;
constexpr int L_WO = 49152;
constexpr int L_FF = 2 * 49152;
constexpr int L_XS = L_FF, L_PART = L_FF + 2560;
constexpr int L_XRAW = 3 * 49152;
constexpr int L_P = L_XRAW + 1280;
constexpr int L_REC = L_P + 1024;
constexpr int L_QS = L_REC + 8 * 416;
constexpr int L_HS = L_QS + 384;
constexpr int L_ACT = L_HS + 384;
constexpr int L_WMAX = L_ACT + 80, L_LSUM = L_WMAX + 64;
constexpr int L_WRE = L_LSUM + 64;
constexpr int L_CTL = L_WRE + 64;
constexpr int L_END = L_CTL + 128;
static_assert(L_END <= 163840, "lds");
#define ADL 20
#define ALT 190
#define ALD 40
#define ADS 40
#define FSL 20
#define D4 66
constexpr unsigned NK = NST - 1;
constexpr unsigned SPIN_CAP = 1u << 18;
#define HOLD 30
#define PD 182
#define PDB 171
#define PDE 186
#define WA 0
#define WF 0
#define OM 12
#define F1D ((PAR(6, TY) >> 8) & 255)
#define F1S ((PAR(6, TY) >> 16) & 255)
#define W2T ((PAR(6, TY) >> 24) & 127)
#define TP 0
#define TL 36
#define WQD ((PAR(4, FL) >> 8) & 255)
#define MS 50
#define PW 10
#define C0T 340
#define C1T 173
#define LBX 190
#define LBT ((PAR(5, TD) >> 10) & 255)
#define LB0 ((PAR(5, TD) >> 18) & 15)
#define LB1 ((PAR(5, TD) >> 22) & 31)
#define LB2 ((PAR(5, TD) >> 27) & 15)
#define NE ((PAR(4, FL) & 3))
#define QT ((PAR(0, P0) >> 8) & 1023)
#define QG (PAR(0, P0) >> 18)
#define QW 18
#define QR 18
#define SRD 165
#define SDT QT
#define SLK (QG & 31)
#define SLV (QG >> 5)
#define SPW 0
#define V0 0
#define SRV 150
#define K0 1
#define PT 120
#define RH 150
#define HS 2
#define GK (PAR(4, FL) >> 16)
#define QM 9
#define PAR(i, dflt) (dflt)
#define RTL()
constexpr int WS_STAMP = 1 << 22;
static_assert(WS_END <= WS_STAMP, "ws");
#define STAMP(i)
#define CNT(e) (void)(e)

typedef unsigned short u16;
typedef unsigned u32x2 __attribute__((ext_vector_type(2)));
typedef unsigned u32x4 __attribute__((ext_vector_type(4)));
typedef float f32x2 __attribute__((ext_vector_type(2)));
typedef float f32x4 __attribute__((ext_vector_type(4)));
typedef __attribute__((ext_vector_type(2))) __bf16 bf16x2;
typedef __amdgpu_buffer_rsrc_t rsrc_t;

__device__ __forceinline__ float lo16(unsigned u) { return __uint_as_float(u << 16); }
__device__ __forceinline__ float hi16(unsigned u) { return __uint_as_float(u & 0xFFFF0000u); }
__device__ __forceinline__ unsigned pk2(float a, float b) {
  f32x2 v = {a, b};
  return __builtin_bit_cast(unsigned, __builtin_convertvector(v, bf16x2));
}
__device__ __forceinline__ float bfr(float f) { return __uint_as_float(pk2(0.f, f)); }
__device__ __forceinline__ float dot2(unsigned a, unsigned b, float acc) {
  return __builtin_amdgcn_fdot2_f32_bf16(__builtin_bit_cast(bf16x2, a), __builtin_bit_cast(bf16x2, b), acc, false);
}
template <int CTRL> __device__ __forceinline__ float dppf(float x) {
  return __builtin_bit_cast(float, __builtin_amdgcn_mov_dpp(__builtin_bit_cast(int, x), CTRL, 0xf, 0xf, true));
}
constexpr int XOR1 = 0xB1, XOR2 = 0x4E, HMIR = 0x141, MIR = 0x140, ROR8 = 0x128;
__device__ __forceinline__ float sw16(float a, float b) {
  auto s = __builtin_amdgcn_permlane16_swap(__float_as_uint(a), __float_as_uint(b), false, false);
  return __uint_as_float(s[0]) + __uint_as_float(s[1]);
}
__device__ __forceinline__ float sw32(float a, float b) {
  auto s = __builtin_amdgcn_permlane32_swap(__float_as_uint(a), __float_as_uint(b), false, false);
  return __uint_as_float(s[0]) + __uint_as_float(s[1]);
}
__device__ __forceinline__ float red16(float x) {
  x += dppf<XOR1>(x); x += dppf<XOR2>(x); x += dppf<HMIR>(x); x += dppf<MIR>(x); return x;
}
__device__ __forceinline__ float red32(float x) { x = red16(x); return sw16(x, x); }
__device__ __forceinline__ float red64(float x) { x = red32(x); return sw32(x, x); }
__device__ __forceinline__ float max16(float x) {
  x = fmaxf(x, dppf<XOR1>(x)); x = fmaxf(x, dppf<XOR2>(x)); x = fmaxf(x, dppf<HMIR>(x)); x = fmaxf(x, dppf<MIR>(x)); return x;
}
__device__ __forceinline__ float max64(float x) {
  x = max16(x);
  auto s = __builtin_amdgcn_permlane16_swap(__float_as_uint(x), __float_as_uint(x), false, false);
  x = fmaxf(__uint_as_float(s[0]), __uint_as_float(s[1]));
  auto t = __builtin_amdgcn_permlane32_swap(__float_as_uint(x), __float_as_uint(x), false, false);
  return fmaxf(__uint_as_float(t[0]), __uint_as_float(t[1]));
}
template <int CTRL> __device__ __forceinline__ float rs(float a, float b, bool sel) {
  const float keep = sel ? b : a, send = sel ? a : b;
  return keep + dppf<CTRL>(send);
}
__device__ __forceinline__ unsigned plo(unsigned r0, unsigned r1) { return __builtin_amdgcn_perm(r1, r0, 0x05040100u); }
__device__ __forceinline__ unsigned phi(unsigned r0, unsigned r1) { return __builtin_amdgcn_perm(r1, r0, 0x07060302u); }
__device__ __forceinline__ rsrc_t mk(const void* p, int bytes) {
  return __builtin_amdgcn_make_buffer_rsrc(const_cast<void*>(p), 0, bytes, 0x00020000);
}
#define BAR() asm volatile("s_waitcnt lgkmcnt(0)\n\ts_barrier" ::: "memory")
#define PIN() __builtin_amdgcn_sched_barrier(0)
constexpr int SC1 = 16, NT = 2, OOB = 0x40000000;

template <int AUX = SC1>
__device__ __forceinline__ void put_unit(rsrc_t rw, int off, float v0, float v1, unsigned ta, unsigned tb) {
  u32x4 u = {__float_as_uint(v0), ta, __float_as_uint(v1), tb};
  __builtin_amdgcn_raw_buffer_store_b128(u, rw, off, 0, AUX);
}
template <int N, int STRIDE, int AUX = SC1>
__device__ __forceinline__ void load_units(rsrc_t rw, int voff, u32x4 (&v)[N]) {
#pragma unroll
  for (int k = 0; k < N; ++k) v[k] = __builtin_amdgcn_raw_buffer_load_b128(rw, voff, k * STRIDE, AUX);
}
template <int N>
__device__ __forceinline__ unsigned units_bad(const u32x4 (&v)[N], unsigned ta, unsigned tb, unsigned acc = 0u) {
  unsigned acc2 = 0u;
#pragma unroll
  for (int k = 0; k < N; ++k) { acc = __builtin_amdgcn_bitop3_b32(v[k].y, ta, acc, 0xBE); acc2 = __builtin_amdgcn_bitop3_b32(v[k].w, tb, acc2, 0xBE); }
  return acc | acc2;
}
__device__ __forceinline__ bool none(bool p) { return __builtin_amdgcn_ballot_w64(p) == 0ull; }
#define RARE(c) __builtin_expect(!!(c), 0)
template <int N, int STRIDE, int AUX = SC1>
__device__ __forceinline__ unsigned poll_units(rsrc_t rw, int voff, u32x4 (&v)[N], unsigned ta, unsigned tb, bool live = true) {
  unsigned spins = 0;
#pragma unroll 1
  for (; spins < SPIN_CAP; ++spins) {
    asm volatile("" : "+v"(voff) :: "memory");
    load_units<N, STRIDE, AUX>(rw, voff, v);
    if (none(units_bad<N>(v, ta, tb) != 0u && live)) break;
  }
  return spins;
}
__device__ __forceinline__ unsigned lds_ld(unsigned* p) {
  return __builtin_amdgcn_readfirstlane(__hip_atomic_load(p, __ATOMIC_RELAXED, __HIP_MEMORY_SCOPE_WORKGROUP));
}
typedef __attribute__((address_space(3))) unsigned* lds_u32;
__device__ __forceinline__ void lds_inc(unsigned* p, int lane, unsigned n = 1u) {
  asm volatile("s_waitcnt lgkmcnt(0)\n\ts_mov_b64 exec, 1\n\tds_add_u32 %0, %1\n\ts_mov_b64 exec, -1" :: "v"((unsigned)(size_t)(lds_u32)p), "v"(n) : "memory");
}
__device__ __forceinline__ void pv_pair(float (&a)[24], const u32x4* A, const u32x4* B, const unsigned (&xp)[3]) {
#pragma unroll
  for (int jj = 0; jj < 3; ++jj)
#pragma unroll
    for (int i = 0; i < 4; ++i) {
      a[8 * jj + 2 * i] = dot2(plo(A[jj][i], B[jj][i]), xp[jj], a[8 * jj + 2 * i]);
      a[8 * jj + 2 * i + 1] = dot2(phi(A[jj][i], B[jj][i]), xp[jj], a[8 * jj + 2 * i + 1]);
    }
}
__device__ __forceinline__ f32x4 red_rows(const float (&a)[24], bool t1) {
  float bq[12], v[6];
#pragma unroll
  for (int k = 0; k < 12; ++k) bq[k] = sw32(a[k], a[k + 12]);
#pragma unroll
  for (int k = 0; k < 6; ++k) { v[k] = sw16(bq[k], bq[k + 6]); v[k] += dppf<ROR8>(v[k]); }
  return f32x4{t1 ? v[4] : v[0], t1 ? v[5] : v[1], v[2], v[3]};
}
template <int N, int STRIDE>
__device__ __forceinline__ unsigned poll_lane(rsrc_t rw, int voff, u32x4 (&v)[N], unsigned ta, unsigned tb) {
  unsigned spins = 0;
#pragma unroll 1
  for (; spins < SPIN_CAP; ++spins) {
    if (none(units_bad<N>(v, ta, tb) != 0u)) break;
#pragma unroll
    for (int k = 0; k < N; ++k) {
      const unsigned long long em = __builtin_amdgcn_ballot_w64(((v[k].y ^ ta) | (v[k].w ^ tb)) != 0u);
      asm volatile("s_mov_b64 exec, %3\n\tbuffer_load_dwordx4 %0, %1, %2, %4 offen sc1\n\ts_mov_b64 exec, -1" : "+v"(v[k]) : "v"(voff), "s"(rw), "s"(em), "s"(k * STRIDE) : "memory");
    }
    if constexpr (N == 2) asm volatile("s_waitcnt vmcnt(0)" : "+v"(v[0]), "+v"(v[1]) :: "memory");
    else asm volatile("s_waitcnt vmcnt(0)" : "+v"(v[0]), "+v"(v[1]), "+v"(v[2]), "+v"(v[3]) :: "memory");
  }
  return spins;
}
#define SL_ "s_sleep 2\n\t"
#define RING(p, t, em) asm volatile("s_mov_b64 exec, %2\n\tglobal_atomic_swap %0, %1, off sc1\n\ts_mov_b64 exec, -1" :: "v"(p), "v"(t), "s"(em) : "memory")
__device__ __forceinline__ void wait_marks(const void* p, unsigned tg, unsigned long long em, unsigned (&s)[4]) {
  unsigned cnt = SPIN_CAP / 4;
#define A1_(a) "global_atomic_add %[" #a "], %[p], %[z], off sc0 sc1\n\t"
#define CH_(a) "s_waitcnt vmcnt(3)\n\tv_cmpx_ne_u32 exec, %[" #a "], %[tg]\n\ts_cbranch_execz 2f\n\t" SL_
  asm volatile(
      "s_mov_b64 exec, %[em]\n\t"
      A1_(a) SL_ A1_(b) SL_ A1_(c) SL_
      "1:\n\t"
      A1_(d) CH_(a)
      A1_(a) CH_(b)
      A1_(b) CH_(c)
      A1_(c) CH_(d)
      "s_sub_u32 %[cnt], %[cnt], 1\n\t"
      "s_cmp_lg_u32 %[cnt], 0\n\t"
      "s_cbranch_scc1 1b\n\t"
      "2:\n\t"
      "s_mov_b64 exec, -1"
      : [cnt] "+s"(cnt), [a] "=&v"(s[0]), [b] "=&v"(s[1]), [c] "=&v"(s[2]), [d] "=&v"(s[3])
      : [p] "v"(p), [z] "v"(0u), [tg] "v"(tg), [em] "s"(em)
      : "memory", "vcc", "scc");
}
__device__ __forceinline__ void announce(const void* p, unsigned tg, unsigned t, unsigned long long em) {
  asm volatile("s_mov_b64 exec, %2\n\tglobal_atomic_swap %0, %1, off sc1\n\ts_mov_b64 exec, -1" :: "v"(p), "v"((tg << 12) | (t & 0xFFFu)), "s"(em) : "memory");
}
__device__ __forceinline__ unsigned gate(const void* p, unsigned tg, unsigned long long em, unsigned (&s)[4]) {
  unsigned cnt = SPIN_CAP / 4, res = 0u, t;
#define CI_(a) "s_waitcnt vmcnt(3)\n\tv_mov_b32 %[res], %[" #a "]\n\tv_lshrrev_b32 %[t], 12, %[" #a "]\n\tv_cmpx_ne_u32 exec, %[t], %[tg]\n\ts_cbranch_execz 2f\n\ts_sleep 1\n\t"
  asm volatile(
      "s_mov_b64 exec, %[em]\n\t"
      A1_(a) SL_ A1_(b) SL_ A1_(c) SL_
      "1:\n\t"
      A1_(d) CI_(a)
      A1_(a) CI_(b)
      A1_(b) CI_(c)
      A1_(c) CI_(d)
      "s_sub_u32 %[cnt], %[cnt], 1\n\t"
      "s_cmp_lg_u32 %[cnt], 0\n\t"
      "s_cbranch_scc1 1b\n\t"
      "2:\n\t"
      "s_mov_b64 exec, -1"
      : [cnt] "+s"(cnt), [res] "+&v"(res), [t] "=&v"(t), [a] "=&v"(s[0]), [b] "=&v"(s[1]), [c] "=&v"(s[2]), [d] "=&v"(s[3])
      : [p] "v"(p), [z] "v"(0u), [tg] "v"(tg & 0xFFFFFu), [em] "s"(em)
      : "memory", "vcc", "scc");
  const unsigned now = (unsigned)__builtin_amdgcn_s_memrealtime();
  const float age = max64(-(float)(res ? (now - res) & 0xFFFu : 0xFFFu));
  return now + (unsigned)__builtin_amdgcn_readfirstlane((int)age);
}
__device__ __forceinline__ void until(unsigned due) {
  while (due - (unsigned)__builtin_amdgcn_s_memrealtime() - 1u < 400u) {}
}
#define KEEP(s) asm volatile("" :: "v"(s[0]), "v"(s[1]), "v"(s[2]), "v"(s[3]) : "memory")
typedef short s16x4 __attribute__((ext_vector_type(4)));
typedef __attribute__((ext_vector_type(8))) __bf16 bf16x8;
typedef __attribute__((address_space(3))) s16x4* lds_s16x4;
__device__ __forceinline__ u32x4 frag(unsigned a, int off, int off_hi) {
  const s16x4 lo = __builtin_amdgcn_ds_read_tr16_b64_v4i16((lds_s16x4)(size_t)(a + off));
  const s16x4 hi = __builtin_amdgcn_ds_read_tr16_b64_v4i16((lds_s16x4)(size_t)(a + off_hi));
  const u32x2 l2 = __builtin_bit_cast(u32x2, lo), h2 = __builtin_bit_cast(u32x2, hi);
  return u32x4{l2.x, l2.y, h2.x, h2.y};
}
__device__ __forceinline__ f32x4 mma(u32x4 a, u32x4 b, f32x4 c) {
  return __builtin_amdgcn_mfma_f32_16x16x32_bf16(__builtin_bit_cast(bf16x8, a), __builtin_bit_cast(bf16x8, b), c, 0, 0, 0);
}
typedef const __attribute__((address_space(3))) u32x4* lds_cv4;
__device__ __forceinline__ unsigned lds_addr(const void* p) { return (unsigned)(size_t)(const __attribute__((address_space(3))) void*)p; }
__device__ __forceinline__ u32x4 lds_ld4(unsigned a, int off) { return *(lds_cv4)(size_t)(a + off); }
__device__ __forceinline__ void lds_st(unsigned a, unsigned v) { *(lds_u32)(size_t)a = v; }
#define PINV(x) asm volatile("" : "+v"(x))
#define WAITC(i, n) while (lds_ld(&ctl[i]) < (n)) __builtin_amdgcn_s_sleep(1); asm volatile("" ::: "memory")
#define WAITS(i, n) while (lds_ld(&ctl[i]) < (n)) __builtin_amdgcn_s_sleep(4); asm volatile("" ::: "memory")
#define SPINC(i, n) while (lds_ld(&ctl[i]) < (n)) {} asm volatile("" ::: "memory")

__device__ __forceinline__ void dma1(rsrc_t r, int voff, int soff, unsigned lds) {
  asm volatile("s_mov_b32 m0, %0\n\ts_nop 0\n\tbuffer_load_dwordx4 %1, %2, %3 offen nt lds" :: "s"(lds), "v"(voff), "s"(r), "s"(soff) : "memory");
}
__device__ __forceinline__ void dma3(rsrc_t r, int voff, int soff, unsigned lds) {
  asm volatile("s_mov_b32 m0, %0\n\ts_nop 0\n\tbuffer_load_dwordx4 %1, %2, %3 offen nt lds\n\t"
               "s_add_u32 m0, m0, 896\n\ts_nop 0\n\tbuffer_load_dwordx4 %1, %2, %3 offen offset:128 nt lds\n\t"
               "s_add_u32 m0, m0, 896\n\ts_nop 0\n\tbuffer_load_dwordx4 %1, %2, %3 offen offset:256 nt lds" :: "s"(lds), "v"(voff), "s"(r), "s"(soff) : "memory", "scc");
}
#define VMW(n) asm volatile("s_waitcnt vmcnt(" #n ")" ::: "memory")
#define BAR0() asm volatile("s_barrier" ::: "memory")

__device__ __forceinline__ void code_ahead(int i) {
  asm volatile("s_getpc_b64 s[88:89]\n1:\n\t"
               "s_add_u32 s88, s88, .Lkf_begin - 1b\n\ts_addc_u32 s89, s89, -1\n\ts_and_b32 s89, s89, 0xffff\n\t"
               "s_mov_b32 s90, 28672\n\ts_mov_b32 s91, 0x20000\n\t"
               "s_buffer_load_dword s92, s[88:91], %0 offset:0\n\ts_buffer_load_dword s92, s[88:91], %0 offset:64\n\t"
               "s_buffer_load_dword s92, s[88:91], %0 offset:128\n\ts_buffer_load_dword s92, s[88:91], %0 offset:192\n\t"
               "s_buffer_load_dword s92, s[88:91], %0 offset:256\n\ts_buffer_load_dword s92, s[88:91], %0 offset:320\n\t"
               "s_buffer_load_dword s92, s[88:91], %0 offset:384\n\ts_waitcnt lgkmcnt(0)"
               :: "s"(i * 448) : "memory", "scc", "s88", "s89", "s90", "s91", "s92");
}
__device__ __forceinline__ void survey(rsrc_t rw, int off, int slot, unsigned* cnt, unsigned c0, unsigned dc, unsigned t0, unsigned dt, unsigned ta, unsigned tb, int lane) {
#define NOW() (unsigned)__builtin_amdgcn_s_memrealtime()
#define PACE(due) { PIN(); const unsigned d_ = (due); while ((int)(NOW() - d_) < 0) __builtin_amdgcn_s_sleep(1); PIN(); }
  if (slot >= 0) {
    unsigned a = 0u;
    int l0 = 0, l1 = 0;
#pragma unroll 1
    for (unsigned i = 0; i < 5; ++i) {
      if (i < 4) while (lds_ld(cnt) < c0 + i * dc) __builtin_amdgcn_s_sleep(1);
      asm volatile("buffer_load_dword %0, %1, %2, 0 offen\n\ts_waitcnt vmcnt(1)" : "+v"(a) : "v"(OOB), "s"(rw) : "memory");
      const int l = min((int)(NOW() - t0 - (i - 1) * dt / 4), 130);
      if (i >= 3) l1 += l; else if (i) l0 += l;
    }
    put_unit(rw, lane < 8 ? off + slot * 128 + lane * 16 : OOB, 0.5f * (float)l0, 0.5f * (float)l1, ta, tb);
    asm volatile("s_waitcnt vmcnt(1)" :: "v"(a) : "memory");
  }
}
__device__ __forceinline__ f32x2 result(rsrc_t rw, int off, unsigned t2, unsigned ta, unsigned tb, int lane) {
  PACE(t2)
  u32x4 v[1];
  load_units<1, 0>(rw, off + (lane & 31) * 128, v);
  const unsigned u0 = v[0].x, u1 = v[0].z;
  const bool ok = units_bad<1>(v, ta, tb) == 0u;
  const float x = red32(ok ? __uint_as_float(lane < 32 ? u0 : u1) : 0.f), n = red32(ok ? 1.f : 0.f);
  const float m = n < 16.f ? 1000.f : x * __builtin_amdgcn_rcpf(n);
  return f32x2{__uint_as_float(__builtin_amdgcn_readlane(__float_as_uint(m), 0)), __uint_as_float(__builtin_amdgcn_readlane(__float_as_uint(m), 32))};
}
__global__ __launch_bounds__(NTHR, 4) void k_fused(const u16* x_, const u16* kc_, const u16* wq_, unsigned char* ws_, const u16* vc_, const u16* wo_,
                                                   const u16* w1_, const u16* w3_, const u16* w2_, u16* out_
) {
  typedef unsigned u32x8 __attribute__((ext_vector_type(8)));
  typedef __attribute__((address_space(1))) unsigned char* gp_t;
  const unsigned long long kap = (unsigned long long)__builtin_amdgcn_kernarg_segment_ptr();
  u32x8 ka, kb;
  u32x4 kc4;
  asm volatile(".Lkf_begin:\n\ts_load_dword s95, %0, 0x0" :: "s"(kap) : "memory", "s95");
#define ARGS() asm volatile("s_load_dwordx8 %0, %1, 0x0\n\ts_waitcnt lgkmcnt(0)" : "=&s"(ka) : "s"(kap) : "memory", "s95")
#define ARG(v, i) ((gp_t)(((unsigned long long)v[2 * (i) + 1] << 32) | v[2 * (i)]))
  __shared__ __attribute__((aligned(1024))) unsigned char smem[L_END];
  float* const xs = reinterpret_cast<float*>(smem + L_XS);
  unsigned* const xraw = reinterpret_cast<unsigned*>(smem + L_XRAW);
  f32x4* const part = reinterpret_cast<f32x4*>(smem + L_PART);
  unsigned* const qs = reinterpret_cast<unsigned*>(smem + L_QS);
  unsigned* const hs = reinterpret_cast<unsigned*>(smem + L_HS);
  float* const wmax = reinterpret_cast<float*>(smem + L_WMAX);
  float* const lsum = reinterpret_cast<float*>(smem + L_LSUM);
  unsigned* const ctl = reinterpret_cast<unsigned*>(smem + L_CTL);
  const unsigned lb = lds_addr(smem);
  STAMP(0);
  __builtin_amdgcn_s_setprio(3);
  const float L2E = 1.4426950408889634f;
  const int tid = threadIdx.x, lane = tid & 63, r8 = lane >> 3, c8 = lane & 7;
  int w = __builtin_amdgcn_readfirstlane(tid >> 6);
  const int b = blockIdx.x, xcd = b & 7, j = b >> 3, h = xcd + 8 * (j >> 3), s = j & 7;
  const bool lt = xcd & 1;
  const bool one = lt || j < 16;
#define BLK(i) ((h * SK + (SK - WIN) + ((i) * 8 + s) * 32) * (DH * 2))
#define LDK() { const rsrc_t rk = mk((const void*)ARG(ka, 1), H * SK * DH * 2); \
    _Pragma("unroll") for (int g = 0; g < 4; ++g) _Pragma("unroll") for (int jj = 0; jj < 3; ++jj) \
    K[3 * g + jj] = __builtin_amdgcn_raw_buffer_load_b128(rk, vkv + jj * 128, sb + g * 3072, NT); }
#define SHIFT(hd, r, t) (((int)(((hd) * (r) / 32u + (r) - (((t) & 0xFFFFFFFu) * 4u) % (r)) % (r)) - (int)(r) / 2) / 4)
#define DUE(n) (tz + (n) * pd / 4)
#define KPACE(n) { tz = lds_ld(&ctl[30]); tz += SHIFT(h, (unsigned)pd, tz) + (PAR(0, P0) & 255); PACE(DUE(n) - 8) PACE(DUE(n) + lds_ld(&ctl[6]) + (n) * lds_ld(&ctl[7]) / 4) }
  unsigned ta = 0u, tb = 0u;
  unsigned tz;
  const int pw = (w + 14) & 15, vkv = r8 * 384 + c8 * 16, sb = BLK(pw);
  const bool t1 = (lane >> 3) & 1;
#define PAIRS() const int k0 = 6 * (lane >> 4) + ((lane >> 1) & 4), k2 = (lane & 8) ? k0 : k0 + 2; \
  const int pr0 = 32 * (k0 >> 3) + 4 * c8 + ((k0 & 7) >> 1), pr1 = 32 * (k2 >> 3) + 4 * c8 + ((k2 & 7) >> 1)
  if (tid < 30) ctl[tid] = 0u;
  int sq[3], sg = w < 2 ? 8 * (H * DH * 2) : 3072, vq = r8 * (sg >> 3) + c8 * 16;
#pragma unroll
  for (int jj = 0; jj < 3; ++jj) {
    const int c = 6 * xcd + 3 * w + jj;
    int t = (c & 15) * 40 * (H * DH * 2) + j * (DH * 2) + (c >> 4) * 128;
    asm volatile("" : "+s"(t));
    sq[jj] = w < 2 ? t : sb + jj * 128;
    asm volatile("" : "+s"(sq[jj]));
  }
  asm volatile("" : "+s"(sg));
  int vq4 = w < 2 ? vq : OOB, vx = w < 2 ? lane * 4 : OOB;
  PINV(vq); PINV(vq4); PINV(vx);
  unsigned xr[5];
  u32x4 W[15];
#define K W
  if (w >= 2) __builtin_amdgcn_s_setprio(2);
  PIN();
  ARGS();
  if (w == 15) ctl[30] = (unsigned)__builtin_amdgcn_s_memrealtime();
  asm volatile("s_waitcnt lgkmcnt(0)\n\ts_cmp_lt_u32 %0, 2\n\ts_cbranch_scc1 1f\n\ts_barrier\n\t1:" : "+s"(w) :: "scc", "memory");
  if (w < 3) {
    const rsrc_t rx = mk((const void*)ARG(ka, 0), D * 2), rq = mk((const void*)(w < 2 ? ARG(ka, 2) : ARG(ka, 1)), w < 2 ? D * H * DH * 2 : H * SK * DH * 2);
#pragma unroll
    for (int k = 0; k < 5; ++k) xr[k] = __builtin_amdgcn_raw_buffer_load_b32(rx, vx + k * 256, 0, 0);
    PIN();
#pragma unroll
    for (int g = 0; g < 5; ++g) {
#pragma unroll
      for (int jj = 0; jj < 3; ++jj)
        W[3 * g + jj] = __builtin_amdgcn_raw_buffer_load_b128(rq, g < 4 ? vq : vq4, sq[jj] + g * sg, NT);
      PIN();
    }
  }
  asm volatile("s_cmp_ge_u32 %0, 2\n\ts_cbranch_scc1 1f\n\ts_barrier\n\t1:" : "+s"(w) :: "scc", "memory");
  unsigned long long eps;
  asm volatile("s_cmp_lg_u32 %2, 12\n\ts_cbranch_scc1 1f\n\ts_load_dwordx2 %0, %1, 0x0\n\ts_waitcnt lgkmcnt(0)\n\t1:" : "=&s"(eps) : "s"(ARG(ka, 3)), "s"(w) : "memory", "scc");
  if (w >= 14) code_ahead(2 * j + (w & 1));
  if (w > 2 && pw < QM) {
    RTL();
    const int pd = (one ? PAR(1, KD) : PAR(2, KDB)) & 255;
    KPACE(pw)
    LDK()
    PIN();
    lds_inc(&ctl[4], lane);
#define SLOT() (s == ((h * 5) & 7) ? h : -1)
    if (pw >= K0 && pw < K0 + 4 && SLOT() >= 0) {
      VMW(0);
      lds_inc(&ctl[14 + (pw - K0) / 2], lane, min((int)(NOW() - DUE(pw)), 130));
      if (pw == K0 + 3) {
        WAITC(5, 1u);
        put_unit(mk((unsigned char*)ARG(ka, 3), WS_END), lane < 8 ? WS_LB + h * 128 + lane * 16 : OOB, 0.5f * (float)lds_ld(&ctl[14]), 0.5f * (float)lds_ld(&ctl[15]), lds_ld(&ctl[0]), lds_ld(&ctl[1]));
      }
    }
  }
  asm volatile("" : "+s"(w));
  if (w < 2) {
    unsigned char* ws = (unsigned char*)ARG(ka, 3);
    const rsrc_t rw = mk(ws, QS == 5120 ? WS_END : 200 << 20);
    __builtin_amdgcn_s_setprio(0);
    PAIRS();
    if (w == 1 && lane < 56) hs[40 + lane] = (lane >= 8 && lane < 48) ? 0x3F803F80u : 0u;
    PIN();
    {
      float sm = 0.f;
#pragma unroll
      for (int k = 0; k < 5; ++k) sm += lo16(xr[k]) + hi16(xr[k]);
      const float mean = red64(sm) * (1.f / D);
      float q2 = 0.f;
#pragma unroll
      for (int k = 0; k < 5; ++k) { const float a = lo16(xr[k]) - mean, bb = hi16(xr[k]) - mean; q2 += a * a + bb * bb; }
      const float var = bfr(red64(q2) * (1.f / D));
      const float mb = bfr(mean), den = bfr(sqrtf(bfr(var + 1e-5f)));
#pragma unroll
      for (int k = 0; k < 5; ++k) {
        f32x2 v = {bfr(bfr(lo16(xr[k]) - mb) / den), bfr(bfr(hi16(xr[k]) - mb) / den)};
        *reinterpret_cast<f32x2*>(&xs[2 * (lane + 64 * k)]) = v;
        xraw[lane + 64 * k] = xr[k];
      }
    }
    __builtin_amdgcn_fence(__ATOMIC_RELEASE, "wavefront");
    __builtin_amdgcn_wave_barrier();
    STAMP(12);
    {
      unsigned x01[3], x23[3], x4[3];
#pragma unroll
      for (int jj = 0; jj < 3; ++jj) {
        const float* xp = &xs[((6 * xcd + 3 * w + jj) & 15) * 40 + r8];
        x01[jj] = pk2(xp[0], xp[8]); x23[jj] = pk2(xp[16], xp[24]); x4[jj] = pk2(xp[32], 0.f);
      }
      float a[24];
#pragma unroll
      for (int k = 0; k < 24; ++k) a[k] = 0.f;
      pv_pair(a, &W[0], &W[3], x01);
      pv_pair(a, &W[6], &W[9], x23);
      pv_pair(a, &W[12], &W[12], x4);
      const f32x4 r4 = red_rows(a, t1);
      f32x2* const pp2 = reinterpret_cast<f32x2*>(part + 48 * w);
      if (!t1) pp2[pr1] = f32x2{r4.z, r4.w};
      pp2[pr0] = f32x2{r4.x, r4.y};
    }
    lds_inc(&ctl[2], lane);
    if (!w) {
      WAITC(5, 1u);
      ta = lds_ld(&ctl[0]); tb = lds_ld(&ctl[1]);
      while (lds_ld(&ctl[2]) != 2u) {}
      asm volatile("" ::: "memory");
      const int cl = (6 * xcd >> 4) + (lane >> 5);
      f32x2 a2 = {0.f, 0.f};
#pragma unroll
      for (int i = 0; i < 6; ++i) { const f32x2 t = reinterpret_cast<const f32x2*>(part)[32 * i + (lane & 31)]; if (((6 * xcd + i) >> 4) == cl) a2 += t; }
      int qo = cl <= ((6 * xcd + 5) >> 4) ? WS_PQ + j * QS + (xcd + (xcd > 2) + (xcd > 5)) * 512 + lane * 16 : OOB;
      PINV(qo); PINV(a2);
      RTL();
      put_unit<QW>(rw, qo, a2.x, a2.y, ta, tb);
    }
    __builtin_amdgcn_s_setprio(2);
  }
  asm volatile("" : "+s"(w));
  if (w < 2 || w >= QM + 2) {
    RTL();
    const int pd = (one ? PAR(1, KD) : PAR(2, KDB)) & 255;
    if (w == 13 && j == 0) {
      const unsigned long long pr = (unsigned long long)ARG(ka, 3) + WS_END;
      asm volatile("s_load_dword s88, %0, 0x0\n\ts_load_dword s89, %0, 0x2000\n\ts_load_dword s90, %0, 0x5000\n\ts_load_dword s91, %0, 0x7000\n\ts_waitcnt lgkmcnt(0)"
                   :: "s"(pr + ((xcd + 1) & 7) * 256) : "memory", "s88", "s89", "s90", "s91");
      const unsigned t0 = lds_ld(&ctl[30]);
      PACE(t0 + PT)
      int d = 0;
#pragma unroll 1
      for (int i = 0; i < 4; ++i) {
        unsigned long long c0, c1;
        asm volatile("s_memtime %0\n\ts_waitcnt lgkmcnt(0)\n\ts_load_dword s88, %2, %3\n\ts_waitcnt lgkmcnt(0)\n\ts_memtime %1\n\ts_waitcnt lgkmcnt(0)"
                     : "=&s"(c0), "=&s"(c1) : "s"(pr + xcd * 256), "s"((i & 1) * 0x2000 + (i >> 1) * 0x5000) : "memory", "s88");
        const int t = min((int)(c1 - c0), 1200);
        d = -d + t;
      }
      WAITC(5, 1u);
      put_unit(mk((unsigned char*)ARG(ka, 3), WS_END), lane < 8 ? WS_TO + xcd * 128 + lane * 16 : OOB, __uint_as_float(t0), (float)(xcd < 4 ? -d : d), lds_ld(&ctl[0]), lds_ld(&ctl[1]));
    }
#define JUDGE(m, th, sl, lv) const int sl = ((m.y - m.x > th && m.y > 86.5f) || m.x > 500.f) ? 1 : 0, lv = min(max((int)m.x - LBT, 0) * LB0 / 8, 40)
    if (w == 12) {
      eps += 1ull;
      ta = (unsigned)eps; tb = (unsigned)(eps >> 32) ^ 0x9E3779B9u;
      if (lane == 0) { ctl[0] = ta; ctl[1] = tb; }
      lds_inc(&ctl[5], lane);
      if (SPW) put_unit(mk((unsigned char*)ARG(ka, 3), WS_END), (SLOT() >= 0 && lane < 16) ? WS_LB + r8 * 4096 + h * 128 + c8 * 16 : OOB, 0.f, 0.f, ~ta, ~tb);
      const unsigned t0 = lds_ld(&ctl[30]);
      const f32x2 m = result(mk((unsigned char*)ARG(ka, 3), WS_END), WS_LB, t0 + (PAR(0, P0) & 255) + (K0 + 3) * pd / 4 + SRD, ta, tb, lane);
      JUDGE(m, 0.5f * (float)(SDT & 31), sl, lv);
      ctl[6] = lv; ctl[7] = sl * SLK; ctl[25] = lv + 4 * sl * SLK;
    }
    KPACE(w < 2 ? QM + w : w)
    LDK()
    PIN();
    lds_inc(&ctl[4], lane);
  }
#define F_(x) __uint_as_float(x)
  asm volatile("s_load_dwordx8 %0, %2, 0x20\n\ts_load_dwordx4 %1, %2, 0x40\n\ts_waitcnt lgkmcnt(0)" : "=&s"(kb), "=&s"(kc4) : "s"(kap) : "memory");
  RTL();
  asm volatile("" : "+s"(w));
  unsigned char* ws = (unsigned char*)ARG(ka, 3);
  const u16* wo = (const u16*)ARG(kb, 1); const u16* w1 = (const u16*)ARG(kb, 2); const u16* w3 = (const u16*)ARG(kb, 3);
  const u16* w2 = (const u16*)ARG(kc4, 0); u16* out = (u16*)ARG(kc4, 1);
  const rsrc_t rl = mk(ws, QS == 5120 ? WS_END : 200 << 20);
  const int pk = (one ? PAR(1, KD) : PAR(2, KDB)) & 255, pd = (one ? PAR(1, KD) : PAR(2, KDB)) >> 8;
  const int dw = w & 1, csw = (c8 ^ (r8 & 2) ^ (4 * dw)) * 16;
  {
    if (w == 0) {
      __builtin_amdgcn_s_setprio(3);
      u32x4 qv[5], ov[1];
      int qo = WS_PQ + h * QS + lane * 16, oo = WS_TO + c8 * 128;
#pragma unroll 1
      for (unsigned spins = 0; spins < SPIN_CAP; ++spins) {
        asm volatile("" : "+v"(qo), "+v"(oo) :: "memory");
        load_units<5, 1024, QR>(rl, qo, qv);
        load_units<1, 0>(rl, oo, ov);
        if (none(units_bad<1>(ov, ta, tb, units_bad<5>(qv, ta, tb)) != 0u)) break;
      }
      f32x2 v[5];
#pragma unroll
      for (int k = 0; k < 5; ++k) { const unsigned u0 = qv[k].x, u1 = qv[k].z; v[k] = f32x2{__uint_as_float(u0), __uint_as_float(u1)}; }
      const bool hi = lane >= 32;
      const f32x2 c0 = hi ? v[0] : v[0] + v[1], c1 = hi ? v[1] + v[2] : v[2] + v[3], c2 = hi ? v[3] + v[4] : v[4];
      qs[lane] = pk2(sw32(c0.x, c1.x), sw32(c0.y, c1.y));
      qs[64 + (lane & 31)] = pk2(sw32(c2.x, c2.x), sw32(c2.y, c2.y));
      const unsigned o_ = ov[0].x, o0 = __builtin_amdgcn_readfirstlane(o_);
      float od = (float)(int)(o_ - o0);
      od += dppf<XOR1>(od); od += dppf<XOR2>(od); od += dppf<HMIR>(od);
      const unsigned p_ = ov[0].z;
      float vt = __uint_as_float(p_);
      vt += dppf<XOR1>(vt); vt += dppf<XOR2>(vt); vt += dppf<HMIR>(vt);
      asm volatile("s_waitcnt lgkmcnt(0)" ::: "memory");
      ctl[3] = ((o0 + (unsigned)(int)(od * 0.125f)) & ~3u) | (vt > 0.f ? 3u : 1u);
    }
  }
  __builtin_amdgcn_s_setprio(1);
  __builtin_amdgcn_s_waitcnt(0x0F70);
  asm volatile("" :: "v"(W[12]), "v"(W[13]), "v"(W[14]), "v"(xr[0]), "v"(xr[1]), "v"(xr[2]), "v"(xr[3]), "v"(xr[4]));
  unsigned tc;
  int pol;
  float wm;
  {
    const rsrc_t rv = mk((const void*)ARG(kb, 0), H * SK * DH * 2);
    const int vv = r8 * 384 + csw;
#define SH(n) (lds_ld(&ctl[25]) + (n) * lds_ld(&ctl[8]) * (one ? 8 : 7) / 32)
#define SHC() (lds_ld(&ctl[25]) + 15 * lds_ld(&ctl[8]) / 4)
#define LDV(vg) { WAITC(4, NK + 8 * (vg)); \
    int so_ = BLK(4 * (vg)) + dw * 3072; unsigned ld_ = lb + L_RING + ((vg) == 3 ? 0 : (vg)) * 49152 + dw * 3072; \
    _Pragma("unroll 1") for (int i = 4 * (vg); i < 4 * (vg) + 4; ++i) { PACE(DUE(i) - 8) PACE(DUE(i) + SH(i)) \
      dma3(rv, vv, so_, ld_); dma3(rv, vv, so_ + 6144, ld_ + 6144); so_ += 98304; ld_ += 12288; lds_inc(&ctl[4], lane); } \
      }
    unsigned tg;
    while ((tg = lds_ld(&ctl[3])) == 0u) __builtin_amdgcn_s_sleep(2);
    asm volatile("" ::: "memory");
    pol = ((tg & 2u) << 12) ^ ((PAR(4, FL) & 128) * 64);
    tz = tg + SHIFT(h, (unsigned)pk, tg) + (PAR(0, P0) & 255) + GK + 4 * pk;
    tc = tg + (PAR(0, P0) & 255) + GK + 4 * (PAR(1, KD) & 255) + 15 * (PAR(1, KD) >> 8) / 4;
    ta = __builtin_amdgcn_readfirstlane(ctl[0]); tb = __builtin_amdgcn_readfirstlane(ctl[1]);
    float pe;
    {
      const unsigned* qp = &qs[4 * c8];
      u32x4 qf[3];
#pragma unroll
      for (int jj = 0; jj < 3; ++jj) qf[jj] = *reinterpret_cast<const u32x4*>(qp + 32 * jj);
      float d[4];
#pragma unroll
      for (int g = 0; g < 4; ++g) {
        d[g] = 0.f;
#pragma unroll
        for (int jj = 0; jj < 3; ++jj)
#pragma unroll
          for (int i = 0; i < 4; ++i) d[g] = dot2(K[3 * g + jj][i], qf[jj][i], d[g]);
        d[g] += dppf<HMIR>(d[g]);
      }
      const float e0 = rs<XOR2>(d[0], d[2], lane & 2), e1 = rs<XOR2>(d[1], d[3], lane & 2);
      float sc = rs<XOR1>(e0, e1, lane & 1);
      sc = bfr(bfr(sc) * 0.07216878364870322f);
      wm = max64(sc);
      pe = sc;
      if (lane == 0) wmax[w] = wm;
      lds_inc(&ctl[9], lane);
    }
    const int vg = (0x42144403 >> (4 * (w >> 1))) & 15;
    if (vg < 2) LDV(vg)
    if (w == 9) survey(rl, WS_LB + 4096, SLOT(), &ctl[4], NK + 2u + 2u * V0, 2u, DUE(V0) + lds_ld(&ctl[25]), pd, ta, tb, lane);
    while (lds_ld(&ctl[9]) < NST) __builtin_amdgcn_s_sleep(2);
    asm volatile("" ::: "memory");
    u16* const pp = reinterpret_cast<u16*>(smem + L_P) + 32 * pw + 8 * (lane & 3) + r8;
    wm = max16(wmax[lane & 15]);
    pe = __builtin_amdgcn_exp2f((pe - wm) * L2E);
    {
      const float p = bfr(pe);
      *pp = (u16)(__float_as_uint(p) >> 16);
      const float ls = red64(p) * 0.5f;
      if (lane == 0) lsum[w] = ls;
    }
    lds_inc(&ctl[10], lane);
    if (w >= 2) lds_inc(&ctl[16 + (pw >> 2)], lane);
    if (vg == 3) {
      WAITC(10, NST);
      *pp = (u16)pk2(pe * __builtin_amdgcn_rcpf(red16(lsum[lane & 15])), 0.f);
      WAITC(20, 6u);
    }
    if (vg < 4) {
      if (vg >= 2) LDV(vg)
      if (vg == 3) VMW(12); else VMW(0);
      lds_inc(&ctl[16 + vg], lane);
    }
  }
  const bool role4 = one, pvw = w >= 4 && w < 10;
  const int q3 = j & 3;
  const int ho = ((!role4 && q3 == 3) ? xcd : xcd ^ 1) + 8 * (lt ? j >> 3 : (j >> 2) & 3), ob = lt ? j & 7 : role4 ? q3 : (q3 == 3 ? 8 : 4 + 2 * q3);
  const int rg = j < 24 ? xcd : xcd ^ 1, ln = j < 24 ? j : j - 8, n5 = 12 * (xcd >> 1) + j - 16;
  const int g4 = lane >> 4, tq = (lane >> 2) & 3, tp = lane & 3;
#define FRAG(t) (g4 * 1024 + tq * 128 + (((2 * (t) + (tp >> 1)) ^ (tq & 2) ^ (4 * (g4 & 1))) * 16) + (tp & 1) * 8)
  const int vo = r8 * (D * 2) + csw;
  const unsigned n12 = (role4 ? 4u : 8u);
  if (pvw) {
    const int t2 = w - 4;
    unsigned va = lb + L_RING + 2 * g4 * 1024 + (t2 >> 1) * 1024 + FRAG(2 * (t2 & 1)), vb = va ^ 32u, pa = lb + L_P + 16 * g4;
    f32x4 ac0 = {0.f, 0.f, 0.f, 0.f}, ac1 = {0.f, 0.f, 0.f, 0.f};
#define PVB(i) { const u32x4 pf = lds_ld4(pa, 64 * (i)); \
    const u32x4 a0 = frag(va, ((i) & 3) * 12288, ((i) & 3) * 12288 + 512), a1 = frag(vb, ((i) & 3) * 12288, ((i) & 3) * 12288 + 512); \
    ac0 = mma(a0, pf, ac0); ac1 = mma(a1, pf, ac1); }
#define PVD() PINV(ac0); PINV(ac1); PIN();
    WAITC(16, 6u);
    PVB(0) PVB(1) PVB(2) PVB(3) va += 49152; vb += 49152;
    lds_inc(&ctl[20], lane);
    PVD()
    WAITC(17, 6u);
    PVB(4) PVB(5) PVB(6) PVB(7) va += 49152; vb += 49152;
    lds_inc(&ctl[21], lane);
    PVD()
    WAITC(10, NST);
    float rl_ = __builtin_amdgcn_rcpf(red16(lsum[lane & 15]));
    PINV(rl_);
    WAITC(18, 6u);
    PVB(8) PVB(9) PVB(10) PVB(11) va -= 98304; vb -= 98304;
    lds_inc(&ctl[22], lane);
    PVD()
    __builtin_amdgcn_s_setprio(3);
    SPINC(19, 4u);
    PVB(12) PVB(13)
    ac0 *= rl_; ac1 *= rl_;
    u32x4 pf = lds_ld4(pa, 64 * 14), pg = lds_ld4(pa, 64 * 15);
    int p1off = (lane & 14) ? OOB : (at_rec(h) ^ pol) + s * 1024 + (8 * t2 + 4 * (lane & 1) + g4) * 16;
    PINV(ac0); PINV(ac1); PINV(pf); PINV(pg); PINV(p1off); PINV(va); PINV(vb);
    SPINC(23, 2u);
    const u32x4 a0 = frag(va, 2 * 12288, 2 * 12288 + 512), a1 = frag(vb, 2 * 12288, 2 * 12288 + 512);
    const u32x4 a2 = frag(va, 3 * 12288, 3 * 12288 + 512), a3 = frag(vb, 3 * 12288, 3 * 12288 + 512);
    PIN();
    ac0 = mma(a0, pf, ac0); ac1 = mma(a1, pf, ac1);
    ac0 = mma(a2, pg, ac0); ac1 = mma(a3, pg, ac1);
    const unsigned x0 = pk2(ac0.x, ac0.y), z0 = pk2(ac0.z, ac0.w), x1 = pk2(ac1.x, ac1.y), z1 = pk2(ac1.z, ac1.w);
    put_unit(rl, p1off, __uint_as_float((lane & 1) ? x1 : x0), __uint_as_float((lane & 1) ? z1 : z0), ta, tb);
    STAMP(4);
  } else if (w < 2) {
    __builtin_amdgcn_s_setprio(3);
    VMW(0);
    lds_inc(&ctl[23], lane);
    tc += SHC();
    const int lag = (int)((unsigned)__builtin_amdgcn_s_memrealtime() - tc) - (PAR(6, TY) & 255);
    if (!w) ctl[26] = ((tc + (lag > 0 ? lag : 0)) | 1u);
    BAR();
  } else if (w < 4) {
    const rsrc_t ro = mk(wo, H * DH * D * 2);
    const int so_o = ((ho * DH + 8 * dw) * D + 64 * ob) * 2;
    WAITS(21, 6u);
    WAITS(4, NK + 32);
    const unsigned t0 = role4 ? tc + (PAR(7, TW) & 255) + SHC() : DUE(16) + SH(16), dt = role4 ? WQD : pd / 8, dg = role4 ? PAR(7, TW) >> 8 : 0;
    __builtin_amdgcn_s_setprio(3);
#pragma unroll 1
    for (int q = 0; q < (role4 ? 4 : 8); ++q) {
      until((t0 + q * dt + (q ? dg : 0)));
#pragma unroll
      for (int n = 0; n < 3; ++n)
        dma1(ro, vo, so_o + (q >> 2) * 128 + ((q & 3) * 3 + n) * 16 * D * 2, lb + L_WO + (2 * (3 * q + n) + dw) * 1024);
    }
    VMW(3);
    lds_inc(&ctl[28], lane);
    VMW(0);
    BAR0();
  } else if (w == 14) {
    const unsigned s0 = lds_ld(&ctl[25]);
    const f32x2 m = result(rl, WS_LB + 4096, tc - (12 - V0) * (PAR(1, KD) >> 8) / 4 + s0 + SRV, ta, tb, lane);
    JUDGE(m, 0.5f * (float)(SDT >> 5), sl, lv);
    ctl[25] = s0 + lv; ctl[8] = sl * SLV;
  } else if (w == 11) {
    WAITS(10, NST);
    const float lse = fmaf(wm, L2E, __builtin_amdgcn_logf(red16(lsum[lane & 15])));
    WAITS(18, 6u);
    tc += SHC();
    PACE(tc + TP)
    put_unit(rl, lane < 8 ? (at_rec(h) ^ pol) + s * 1024 + 768 + lane * 16 : OOB, lse, lse, ta, tb);
    PACE(tc + TL)
    u32x4 lv_[1];
    poll_units<1, 0>(rl, (at_rec(ho) ^ pol) + c8 * 1024 + 768, lv_, ta, tb);
    const unsigned mu = lv_[0].x;
    const float ms = __uint_as_float(mu);
    float Mg = fmaxf(ms, dppf<XOR1>(ms)); Mg = fmaxf(Mg, dppf<XOR2>(Mg)); Mg = fmaxf(Mg, dppf<HMIR>(Mg));
    const float wg = __builtin_amdgcn_exp2f(ms - Mg);
    float Lg = wg + dppf<XOR1>(wg); Lg += dppf<XOR2>(Lg); Lg += dppf<HMIR>(Lg);
    lds_st(lb + L_WRE + 4 * c8, __float_as_uint(wg * __builtin_amdgcn_rcpf(Lg)));
    lds_inc(&ctl[28], lane);
  }
  __builtin_amdgcn_s_setprio(3);
  if (w >= (role4 ? 12 : 8)) {
    const int bk = w < 12;
    unsigned ba = lb + L_WO + bk * 24576 + FRAG(w & 3);
    unsigned ao = lb + L_REC + c8 * 416 + 16 * g4;
    int so = lane < 8 ? WS_P2 + (at_row(ho, 4 * (ob + bk) + (w & 3)) ^ pol) + lane * 16 : OOB;
    int ro_ = lane < 48 ? (at_rec(ho) ^ pol) + (w & 3) * 2048 + lane * 16 : OOB;
    unsigned ra = lb + L_REC + (w & 3) * 832 + (lane < 48 ? lane * 8 : 384);
    unsigned tl = lane < 48 ? ta : 0u, th = lane < 48 ? tb : 0u;
    PINV(ba); PINV(ao); PINV(so); PINV(ro_); PINV(ra); PINV(tl); PINV(th);
    u32x4 bf[6], af[6], v[2], wu;
    if (!bk) {
      WAITS(26, 1u);
      until(lds_ld(&ctl[26]) + (PAR(3, TB) & 255));
      asm volatile("buffer_load_dwordx4 %0, %2, %3, 0 offen sc1\n\tbuffer_load_dwordx4 %1, %2, %3, 0 offen offset:1024 sc1" : "=&v"(v[0]), "=&v"(v[1]) : "v"(ro_), "s"(rl) : "memory");
      STAMP(15);
#define AHEAD() SPINC(28, 3u);                 \
      _Pragma("unroll") for (int ks = 0; ks < 4; ++ks) bf[ks] = frag(ba, ks * 4096, ks * 4096 + 512); \
      wu = lds_ld4(lb + L_WRE + 16 * (g4 & 1), 0); \
      _Pragma("unroll") for (int ks = 0; ks < 4; ++ks) PINV(bf[ks]); \
      PINV(wu); PIN();
      AHEAD()
      asm volatile("s_waitcnt vmcnt(0)" : "+v"(v[0]), "+v"(v[1]) :: "memory");
#define RECS() _Pragma("unroll") for (int k = 0; k < 2; ++k) *(__attribute__((address_space(3))) u32x2*)(size_t)(ra + k * 416) = u32x2{v[k].x, v[k].z};
      RECS()
      if (RARE(!none(units_bad<2>(v, tl, th) != 0u))) {
        CNT((poll_lane<2, 1024>(rl, ro_, v, tl, th)));
        RECS()
      }
    } else {
      WAITS(28, 1u);
      AHEAD()
    }
    BAR();
    const f32x4 wv = __builtin_bit_cast(f32x4, wu);
#pragma unroll
    for (int ks = 0; ks < 4; ++ks) af[ks] = lds_ld4(ao, 64 * ks);
#pragma unroll
    for (int ks = 4; ks < 6; ++ks) { af[ks] = lds_ld4(ao, 64 * ks); bf[ks] = frag(ba, ks * 4096, ks * 4096 + 512); }
    PIN();
    f32x4 acc = {0.f, 0.f, 0.f, 0.f}, ac2 = {0.f, 0.f, 0.f, 0.f};
#pragma unroll
    for (int ks = 0; ks < 6; ks += 2) { acc = mma(af[ks], bf[ks], acc); ac2 = mma(af[ks + 1], bf[ks + 1], ac2); }
    float c0 = fmaf(wv.w, acc.w, fmaf(wv.z, acc.z, fmaf(wv.y, acc.y, wv.x * acc.x)));
    const float c2 = fmaf(wv.y, ac2.y, wv.x * ac2.x), c3 = fmaf(wv.w, ac2.w, fmaf(wv.z, ac2.z, c0));
    c0 = c2 + c3;
    c0 = sw16(c0, c0);
    const float c1 = dppf<ROR8>(c0);
    put_unit(rl, so, c0, c1, ta, tb);
    PIN();
    lds_inc(&ctl[12], lane);
    STAMP(6);
  }
  if (role4) {
    if ((0x0E0C >> w) & 1) {
      if (w >= 4) BAR0();
      const int hw = w < 4 ? w - 2 : w - 7;
      const int cl = 16 * hw + c8 + 8 * (lane >> 5);
      float xv = lo16(reinterpret_cast<const u16*>(xraw)[80 * rg + cl]);
      unsigned hoa = lb + L_HS + 2 * cl;
      u32x4 v[4];
      int po = WS_P2 + (at_row(r8, 5 * rg + hw) ^ pol) + c8 * 16;
      PINV(xv); PINV(hoa); PINV(po);
      const unsigned th_ = (lds_ld(&ctl[26]) + (PAR(5, TD) & 1023));
      __builtin_amdgcn_s_sleep(HS);
      until(th_);
      load_units<4, 65536>(rl, po, v);
      CNT((poll_lane<4, 65536>(rl, po, v, ta, tb)));
      float s0 = (F_(v[0].x) + F_(v[1].x)) + (F_(v[2].x) + F_(v[3].x));
      const float s1 = (F_(v[0].z) + F_(v[1].z)) + (F_(v[2].z) + F_(v[3].z));
      s0 = sw32(s0, s1);
      s0 = sw16(s0, s0);
      s0 += dppf<ROR8>(s0);
      s0 = bfr(s0) + xv;
      *(__attribute__((address_space(3))) u16*)(size_t)hoa = (u16)pk2(s0, s0);
      BAR();
      STAMP(7);
    } else if (w < 13) {
      if (w >= 4 && w < 9) {
        const rsrc_t r1 = mk(w1, D * F * 2), r3 = mk(w3, D * F * 2);
        const int q = w - 4;
        const int v0 = r8 * (F * 2) + csw, v1 = v0 ^ 64, so = ((80 * rg + 8 * q) * F + ln * 64) * 2;
        const unsigned fd = lb + L_FF + q * 1024;
        WAITC(22, 6u);
        WAITS(26, 1u);
        until((lds_ld(&ctl[26]) + (PAR(3, TB) & 255) + F1D + (q >= NE ? F1S : 0)));
        dma1(r1, v0, so, fd); dma1(r3, v0, so, fd + 10240);
        dma1(r1, v1, so + 40 * F * 2, fd + 5120); dma1(r3, v1, so + 40 * F * 2, fd + 15360);
        BAR0();
        VMW(0);
        lds_inc(&ctl[13], lane);
      } else if (w < 2) {
        WAITC(12, n12);
        if (PAR(4, FL) & 16) PACE(lds_ld(&ctl[26]) + (PAR(5, TD) & 1023) + PW)
        if (!(PAR(4, FL) & 32)) put_unit(rl, (at_g(2 * ln + w) ^ pol) + (rg * 64 + lane) * 16, 0.f, 0.f, ~ta, ~tb);
      }
      const int tw = w < 2 ? w : w < 9 ? w - 2 : 7;
      unsigned bo = lb + L_FF + (tw >> 2) * 10240 + FRAG(tw & 3), b2 = bo + (g4 < 2 ? 8192 : 0);
      int go4 = lane < 16 ? (at_g(2 * ln + ((tw >> 1) & 1)) ^ pol) + (rg * 64 + (tw >> 2) * 32 + (tw & 1) * 16 + lane) * 16 : OOB;
      unsigned ao = lb + L_HS + 192 * (lane & 1) + 16 * g4;
      PINV(go4); PINV(ao); PINV(bo); PINV(b2);
      u32x4 af[3], bf[3];
      WAITC(13, 5u);
      bf[0] = frag(bo, 0, 512); bf[1] = frag(bo, 4096, 4096 + 512); bf[2] = frag(b2, 0, 512);
      PINV(bf[0]); PINV(bf[1]); PINV(bf[2]);
      VMW(0);
      BAR0();
#pragma unroll
      for (int ks = 0; ks < 3; ++ks) af[ks] = lds_ld4(ao, 64 * ks);
      PIN();
      f32x4 acc = {0.f, 0.f, 0.f, 0.f};
#pragma unroll
      for (int ks = 0; ks < 3; ++ks) acc = mma(af[ks], bf[ks], acc);
      put_unit(rl, go4, acc.x, acc.y, ta, tb);
      STAMP(8);
    } else if (w == 13) {
      unsigned char* go = ws + (at_m3(2 * ln + (lane & 1), rg) ^ pol);
      unsigned mt = ta;
      PINV(go); PINV(mt);
      BAR0();
      RING(go, mt, 3ull);
      __builtin_amdgcn_raw_buffer_store_b64(u32x2{ta, tb ^ 0x9E3779B9u}, rl, (b == 0 && lane == 0) ? 0 : OOB, 0, SC1);
      STAMP(12);
    } else if (w == 14) {
      unsigned hi_ = lb + L_HS + 4 * (lane < 40 ? lane : 0);
      int sto = lane < 8 ? (at_st(ln) ^ pol) + rg * 128 + lane * 16 : OOB;
      PINV(hi_); PINV(sto);
      if (PAR(4, FL) & 16) PACE(lds_ld(&ctl[26]) + (PAR(5, TD) & 1023) + PW)
      if (!(PAR(4, FL) & 32)) put_unit(rl, sto, 0.f, 0.f, ~ta, ~tb);
      VMW(0);
      BAR0();
      const unsigned hp = *(lds_u32)(size_t)hi_;
      const float h0 = lane < 40 ? lo16(hp) : 0.f, h1 = lane < 40 ? hi16(hp) : 0.f;
      const float cs = __uint_as_float(__builtin_amdgcn_readfirstlane(hp) << 16);
      const float d0 = lane < 40 ? h0 - cs : 0.f, d1 = lane < 40 ? h1 - cs : 0.f;
      float sr = red32(sw32(d0 + d1, d0 * d0 + d1 * d1));
      const float s2 = __uint_as_float(__builtin_amdgcn_readlane(__float_as_uint(sr), 32));
      const float mw = fmaf(sr, 1.f / 80.f, cs), m2 = fmaxf(fmaf(-sr * (1.f / 80.f), sr, s2), 0.f);
      put_unit(rl, sto, mw, m2, ta, tb);
    } else if (j < 5) {
      const rsrc_t rout = mk(out, (D * 2));
      const int cl = 16 * j + c8 + 8 * (lane >> 5);
      unsigned char* gi = ws + (at_m4(5 * (xcd >> 2) + j, lane < N5 ? lane : 0) ^ pol);
      int op = (lane & 24) ? OOB : (80 * xcd + cl) * 2;
      int po = WS_P5 + (at_row(r8, 5 * xcd + j) ^ pol) + c8 * 16;
      unsigned sl[4];
      PINV(gi); PINV(op); PINV(po);
      BAR0();
      float hv = lo16(reinterpret_cast<const u16*>(hs)[cl]);
      PINV(hv);
      __builtin_amdgcn_s_sleep(FSL);
      until(gate(gi, ta, (1ull << N5) - 1ull, sl) + (PAR(3, TB) >> 8));
      u32x4 v[6];
      load_units<6, 65536>(rl, po, v);
      PIN();
#define FIN_() { float s0 = F_(v[0].x), s1 = F_(v[0].z); bad = 0u; \
        _Pragma("unroll") for (int k = 0; k < 6; ++k) { if (k) { s0 += F_(v[k].x); s1 += F_(v[k].z); } \
          if (k < 5) { bad = __builtin_amdgcn_bitop3_b32(v[k].y, ta, bad, 0xBE); bad = __builtin_amdgcn_bitop3_b32(v[k].w, tb, bad, 0xBE); PINV(s0); PINV(s1); PINV(bad); PIN(); } } \
        s0 = sw32(s0, s1); \
        bad = __builtin_amdgcn_bitop3_b32(v[5].y, ta, bad, 0xBE); bad = __builtin_amdgcn_bitop3_b32(v[5].w, tb, bad, 0xBE); \
        s0 = sw16(s0, s0); s0 += dppf<ROR8>(s0); s0 = bfr(s0) + hv; res = pk2(s0, s0); }
      unsigned res, bad;
      FIN_()
      PINV(res);
      if (RARE(!none(bad != 0u))) {
        CNT((poll_units<6, 65536>(rl, po, v, ta, tb)));
        FIN_()
      }
      __builtin_amdgcn_raw_buffer_store_b16((u16)res, rout, op, 0, 0);
      STAMP(17);
      KEEP(sl);
      asm volatile("s_endpgm");
    }
  } else if (j < 28) {
    if (w == 3) {
      unsigned char* gi = ws + (at_m3(n5, c8) ^ pol);
      unsigned ai = lb + L_ACT + (lane < 32 ? 2 * lane : 64), fa = lb + L_CTL + 24 * 4;
      int go_ = (at_g(n5) ^ pol) + lane * 16, so_ = (at_st(n5 >> 1) ^ pol) + c8 * 128;
      unsigned sl[4], lvv = 1u;
      PINV(gi); PINV(ai); PINV(fa); PINV(go_); PINV(so_); PINV(lvv);
      WAITC(12, n12);
      if (PAR(4, FL) & 8) PACE(lds_ld(&ctl[26]) + (PAR(5, TD) & 1023) + MS)
      wait_marks(gi, ta, 0xFFull, sl);
      u32x4 gv[8], sv[1];
      load_units<1, 0>(rl, so_, sv);
      load_units<8, 1024>(rl, go_, gv);
      PIN();
      lds_st(fa, lvv);
      PIN();
#define ACT_() { const float c0s = F_(__builtin_amdgcn_readfirstlane(sv[0].x));       \
        float da = F_(sv[0].x) - c0s, m2 = fmaf(80.f * da, da, F_(sv[0].z)); \
        da += dppf<XOR1>(da); m2 += dppf<XOR1>(m2); da += dppf<XOR2>(da); m2 += dppf<XOR2>(m2); da += dppf<HMIR>(da); m2 += dppf<HMIR>(m2); \
        da *= 0.125f; \
        const float var = bfr(fmaxf(fmaf(-da, da, m2 * (1.f / D)), 0.f)); \
        float mb = bfr(c0s + da), rd = __builtin_amdgcn_rcpf(bfr(__builtin_amdgcn_sqrtf(bfr(var + 1e-5f)))); \
        PINV(mb); PINV(rd); PIN(); \
        float gs_ = F_(gv[0].x), cs_ = F_(gv[0].z); \
        bad = __builtin_amdgcn_bitop3_b32(sv[0].y, ta, 0u, 0xBE); bad = __builtin_amdgcn_bitop3_b32(sv[0].w, tb, bad, 0xBE); \
        _Pragma("unroll") for (int k = 0; k < 8; ++k) { if (k) { gs_ += F_(gv[k].x); cs_ += F_(gv[k].z); } \
          if (k < 7) { bad = __builtin_amdgcn_bitop3_b32(gv[k].y, ta, bad, 0xBE); bad = __builtin_amdgcn_bitop3_b32(gv[k].w, tb, bad, 0xBE); PINV(gs_); PINV(cs_); PINV(bad); PIN(); } } \
        float val = bfr((gs_ - mb * cs_) * rd); \
        float sg = bfr(val * __builtin_amdgcn_rcpf(1.f + __builtin_amdgcn_exp2f(-val * L2E))); \
        const float av = sg * F_(__builtin_amdgcn_permlane32_swap(__float_as_uint(val), __float_as_uint(val), false, false)[1]);     \
        *(__attribute__((address_space(3))) u16*)(size_t)ai = (u16)pk2(av, av); \
        bad = __builtin_amdgcn_bitop3_b32(gv[7].y, ta, bad, 0xBE); bad = __builtin_amdgcn_bitop3_b32(gv[7].w, tb, bad, 0xBE); }
      unsigned bad;
      ACT_()
      if (RARE(!none(bad != 0u))) {
        CNT((poll_units<8, 1024>(rl, go_, gv, ta, tb))); poll_units<1, 0>(rl, so_, sv, ta, tb);
        ACT_()
      }
      BAR();
      KEEP(sl);
    } else if (w == 2) {
      unsigned char* go = ws + (at_m4(lane < NG4 ? lane : 0, n5) ^ pol);
      PINV(go);
      WAITC(13, 7u);
      SPINC(24, 1u);
      announce(go, ta, (unsigned)__builtin_amdgcn_s_memrealtime(), ((1ull << NG4) - 1ull));
      BAR0();
    } else if (w >= 4 && w < 14) {
      if (w < 8) {
        const rsrc_t r2 = mk(w2, F * D * 2);
        const int q = w - 4, so = (R5 * n5 + 8 * q) * D * 2;
        const unsigned fd = lb + L_FF + q * 1024;
        if (W2E) {
          WAITC(22, 6u);
          WAITS(26, 1u);
          until((lds_ld(&ctl[26]) + (PAR(3, TB) & 255) + W2T));
#pragma unroll
          for (int i = 0; i < W2E; ++i) dma1(r2, vo, so + i * 128, fd + i * 4096);
        }
        BAR0();
        WAITC(12, n12);
#pragma unroll
        for (int i = W2E; i < 10; ++i) dma1(r2, vo, so + i * 128, fd + i * 4096);
        VMW(0);
        lds_inc(&ctl[13], lane);
      }
      const int fw = w - 4;
      int so = (lane & 8) ? OOB : WS_P5 + (at_row(n5, 4 * fw) ^ pol) + (8 * g4 + c8) * 16;
      WAITC(13, 7u);
      u32x4 bf[4];
#pragma unroll
      for (int t = 0; t < 4; ++t) bf[t] = frag(lb + L_FF + fw * 4096 + FRAG(t), 0, 512);
#pragma unroll
      for (int t = 0; t < 4; ++t) asm volatile("" : "+v"(bf[t]));
      unsigned ao = lb + L_ACT + 16 * g4;
      PINV(ao); PINV(so);
      BAR0();
      const u32x4 av = lds_ld4(ao, 0);
      const f32x4 z = {0.f, 0.f, 0.f, 0.f};
      f32x4 d0 = mma(av, bf[0], z), d1 = mma(av, bf[1], z), d2 = mma(av, bf[2], z), d3 = mma(av, bf[3], z);
      asm volatile("" : "+v"(d0), "+v"(d1), "+v"(d2), "+v"(d3));
      float c0 = __uint_as_float(__builtin_amdgcn_update_dpp(__float_as_uint(d0.x), __float_as_uint(d1.x), 0xE4, 0x2, 0xf, false));
      c0 = __uint_as_float(__builtin_amdgcn_update_dpp(__float_as_uint(c0), __float_as_uint(d2.x), 0xE4, 0x4, 0xf, false));
      c0 = __uint_as_float(__builtin_amdgcn_update_dpp(__float_as_uint(c0), __float_as_uint(d3.x), 0xE4, 0x8, 0xf, false));
      const float c1 = dppf<ROR8>(c0);
      put_unit(rl, so, c0, c1, ta, tb);
      PIN();
      STAMP(10);
    } else {
      if (w != 15) {
        WAITC(12, n12);
        if (PAR(4, FL) & 16) PACE(lds_ld(&ctl[26]) + (PAR(5, TD) & 1023) + PW)
        const int pz = (PAR(4, FL) & 64) ? OOB : WS_P5 + at_row(n5, 0) + (w < 2 ? 8192 * w + lane * 16 : 2048 + (lane >> 5) * 8192 + (lane & 31) * 16);
        put_unit(rl, pz, 0.f, 0.f, ~ta, ~tb);
        put_unit(rl, w < 2 ? pz + 1024 : OOB, 0.f, 0.f, ~ta, ~tb);
        VMW(0);
        lds_inc(&ctl[13], lane);
      }
    }
  }
}
}

extern "C" void kernel_launch(void* const* d_in, const int* in_sizes, int n_in, void* d_out, int out_size, void* d_ws, size_t ws_size,
                              hipStream_t stream) {
  hipLaunchKernelGGL(k_fused, dim3(NWG), dim3(NTHR), 0, stream, (const u16*)d_in[0], (const u16*)d_in[1], (const u16*)d_in[3], (unsigned char*)d_ws,
                     (const u16*)d_in[2], (const u16*)d_in[4], (const u16*)d_in[6], (const u16*)d_in[7], (const u16*)d_in[8], (u16*)d_out
);
}
```

```cpp
#include <hip/hip_runtime.h>
#include <cstdint>

namespace {
constexpr int D = 640, H = 32, DH = 192, SK = 32768, WIN = 4096, F = 1536;
constexpr int NWG = 256, NTHR = 1024;
constexpr int N5 = 48, R5 = F / N5;
constexpr int NST = 16, NREC = 8;
constexpr int WS_PQ = 256;
constexpr int WS_TO = WS_PQ + 320 * 32 * 16;
constexpr int WS_LB = WS_TO + 1024;
constexpr int WS_P1 = 11 * 16384;
constexpr int WS_P2 = WS_P1 + 16 * 16384;
constexpr int WS_G = WS_P2 + 16 * 16384;
constexpr int WS_ST = WS_G + 24 * 16384;
constexpr int WS_P5 = WS_ST + 2 * 16384;
constexpr int NG4 = 10;
constexpr int WS_GT = WS_P5 + 24 * 16384;
constexpr int WS_END = WS_GT + 2 * 16384;
static_assert(WS_LB + 8192 <= WS_P1, "ws");
constexpr int at_rec(int hd) { return WS_P1 + ((hd & 3) + 4 * (hd >> 3)) * 16384 + (hd & 4) * 2048; }
constexpr int at_row(int n, int tile) { return (n >> 1) * 16384 + (n & 1) * 4096 + (n & 6) * 256 + (tile < 20 ? tile * 128 : 8192 + (tile - 20) * 128); }
constexpr int at_g(int n) { return WS_G + (n < 24 ? n * 16384 : 8192 + (n - 24) * 16384); }
constexpr int at_st(int l) { return WS_ST + (l < 12 ? 0 : 8192) + ((l < 12 ? l : l - 12) >> 3) * 16384 + ((l < 12 ? l : l - 12) & 7) * 1024; }
constexpr int at_m3(int n, int i) { return WS_GT + (n < 24 ? n * 256 : 8192 + (n - 24) * 256) + i * 4; }
constexpr int at_m4(int c, int n) { return WS_GT + 16384 + (c < 5 ? c * 768 : 8192 + (c - 5) * 768) + (n >> 4) * 256 + (n & 15) * 4; }
constexpr int L_RING = 0;
constexpr int L_WO = 49152;
constexpr int L_FF = 2 * 49152;
constexpr int L_XS = L_FF, L_PART = L_FF + 2560;
constexpr int L_XRAW = 3 * 49152;
constexpr int L_P = L_XRAW + 1280;
constexpr int L_REC = L_P + 1024;
constexpr int L_QS = L_REC + 8 * 416;
constexpr int L_HS = L_QS + 384;
constexpr int L_ACT = L_HS + 384;
constexpr int L_WMAX = L_ACT + 80, L_LSUM = L_WMAX + 64;
constexpr int L_WRE = L_LSUM + 64;
constexpr int L_CTL = L_WRE + 64;
constexpr int L_END = L_CTL + 128;
static_assert(L_END <= 163840, "lds");
constexpr unsigned NK = NST - 1;
constexpr unsigned SPIN_CAP = 1u << 18;
constexpr int P0 = 98, RK = 164, RV = 167, RV2 = 147, GK = 10, SLK = 15, SLV = 18, THK = 14, THV = 12;
constexpr int LBT = 88, LB0 = 10, SRD = 155, SRV = 150, TW = 70, WQD = 16, TL = 36, TB = 165, TY = 105;
constexpr int F1D = 5, F1S = 20, NE = 2, W2T = 22, TD = 164, PW = 10, D4 = 58, PT = 110, LBK = 70;
constexpr int QM = 9, W2E = 6;

typedef unsigned short u16;
typedef unsigned u32x2 __attribute__((ext_vector_type(2)));
typedef unsigned u32x4 __attribute__((ext_vector_type(4)));
typedef float f32x2 __attribute__((ext_vector_type(2)));
typedef float f32x4 __attribute__((ext_vector_type(4)));
typedef __attribute__((ext_vector_type(2))) __bf16 bf16x2;
typedef __amdgpu_buffer_rsrc_t rsrc_t;

__device__ __forceinline__ float lo16(unsigned u) { return __uint_as_float(u << 16); }
__device__ __forceinline__ float hi16(unsigned u) { return __uint_as_float(u & 0xFFFF0000u); }
__device__ __forceinline__ unsigned pk2(float a, float b) {
  f32x2 v = {a, b};
  return __builtin_bit_cast(unsigned, __builtin_convertvector(v, bf16x2));
}
__device__ __forceinline__ float bfr(float f) { return __uint_as_float(pk2(0.f, f)); }
__device__ __forceinline__ float dot2(unsigned a, unsigned b, float acc) {
  return __builtin_amdgcn_fdot2_f32_bf16(__builtin_bit_cast(bf16x2, a), __builtin_bit_cast(bf16x2, b), acc, false);
}
template <int CTRL> __device__ __forceinline__ float dppf(float x) {
  return __builtin_bit_cast(float, __builtin_amdgcn_mov_dpp(__builtin_bit_cast(int, x), CTRL, 0xf, 0xf, true));
}
constexpr int XOR1 = 0xB1, XOR2 = 0x4E, HMIR = 0x141, MIR = 0x140, ROR8 = 0x128;
__device__ __forceinline__ float sw16(float a, float b) {
  auto s = __builtin_amdgcn_permlane16_swap(__float_as_uint(a), __float_as_uint(b), false, false);
  return __uint_as_float(s[0]) + __uint_as_float(s[1]);
}
__device__ __forceinline__ float sw32(float a, float b) {
  auto s = __builtin_amdgcn_permlane32_swap(__float_as_uint(a), __float_as_uint(b), false, false);
  return __uint_as_float(s[0]) + __uint_as_float(s[1]);
}
__device__ __forceinline__ float red16(float x) {
  x += dppf<XOR1>(x); x += dppf<XOR2>(x); x += dppf<HMIR>(x); x += dppf<MIR>(x); return x;
}
__device__ __forceinline__ float red32(float x) { x = red16(x); return sw16(x, x); }
__device__ __forceinline__ float red64(float x) { x = red32(x); return sw32(x, x); }
__device__ __forceinline__ float max16(float x) {
  x = fmaxf(x, dppf<XOR1>(x)); x = fmaxf(x, dppf<XOR2>(x)); x = fmaxf(x, dppf<HMIR>(x)); x = fmaxf(x, dppf<MIR>(x)); return x;
}
__device__ __forceinline__ float max64(float x) {
  x = max16(x);
  auto s = __builtin_amdgcn_permlane16_swap(__float_as_uint(x), __float_as_uint(x), false, false);
  x = fmaxf(__uint_as_float(s[0]), __uint_as_float(s[1]));
  auto t = __builtin_amdgcn_permlane32_swap(__float_as_uint(x), __float_as_uint(x), false, false);
  return fmaxf(__uint_as_float(t[0]), __uint_as_float(t[1]));
}
template <int CTRL> __device__ __forceinline__ float rs(float a, float b, bool sel) {
  const float keep = sel ? b : a, send = sel ? a : b;
  return keep + dppf<CTRL>(send);
}
__device__ __forceinline__ unsigned plo(unsigned r0, unsigned r1) { return __builtin_amdgcn_perm(r1, r0, 0x05040100u); }
__device__ __forceinline__ unsigned phi(unsigned r0, unsigned r1) { return __builtin_amdgcn_perm(r1, r0, 0x07060302u); }
__device__ __forceinline__ rsrc_t mk(const void* p, int bytes) {
  return __builtin_amdgcn_make_buffer_rsrc(const_cast<void*>(p), 0, bytes, 0x00020000);
}
#define BAR() asm volatile("s_waitcnt lgkmcnt(0)\n\ts_barrier" ::: "memory")
#define PIN() __builtin_amdgcn_sched_barrier(0)
constexpr int SC1 = 16, NT = 2, OOB = 0x40000000;

template <int AUX = SC1>
__device__ __forceinline__ void put_unit(rsrc_t rw, int off, float v0, float v1, unsigned ta, unsigned tb) {
  u32x4 u = {__float_as_uint(v0), ta, __float_as_uint(v1), tb};
  __builtin_amdgcn_raw_buffer_store_b128(u, rw, off, 0, AUX);
}
template <int N, int STRIDE, int AUX = SC1>
__device__ __forceinline__ void load_units(rsrc_t rw, int voff, u32x4 (&v)[N]) {
#pragma unroll
  for (int k = 0; k < N; ++k) v[k] = __builtin_amdgcn_raw_buffer_load_b128(rw, voff, k * STRIDE, AUX);
}
template <int N>
__device__ __forceinline__ unsigned units_bad(const u32x4 (&v)[N], unsigned ta, unsigned tb, unsigned acc = 0u) {
  unsigned acc2 = 0u;
#pragma unroll
  for (int k = 0; k < N; ++k) { acc = __builtin_amdgcn_bitop3_b32(v[k].y, ta, acc, 0xBE); acc2 = __builtin_amdgcn_bitop3_b32(v[k].w, tb, acc2, 0xBE); }
  return acc | acc2;
}
__device__ __forceinline__ bool none(bool p) { return __builtin_amdgcn_ballot_w64(p) == 0ull; }
#define RARE(c) __builtin_expect(!!(c), 0)
template <int N, int STRIDE, int AUX = SC1>
__device__ __forceinline__ unsigned poll_units(rsrc_t rw, int voff, u32x4 (&v)[N], unsigned ta, unsigned tb, bool live = true) {
  unsigned spins = 0;
#pragma unroll 1
  for (; spins < SPIN_CAP; ++spins) {
    asm volatile("" : "+v"(voff) :: "memory");
    load_units<N, STRIDE, AUX>(rw, voff, v);
    if (none(units_bad<N>(v, ta, tb) != 0u && live)) break;
  }
  return spins;
}
__device__ __forceinline__ unsigned lds_ld(unsigned* p) {
  return __builtin_amdgcn_readfirstlane(__hip_atomic_load(p, __ATOMIC_RELAXED, __HIP_MEMORY_SCOPE_WORKGROUP));
}
typedef __attribute__((address_space(3))) unsigned* lds_u32;
__device__ __forceinline__ void lds_inc(unsigned* p, int lane, unsigned n = 1u) {
  asm volatile("s_waitcnt lgkmcnt(0)\n\ts_mov_b64 exec, 1\n\tds_add_u32 %0, %1\n\ts_mov_b64 exec, -1" :: "v"((unsigned)(size_t)(lds_u32)p), "v"(n) : "memory");
}
__device__ __forceinline__ void pv_pair(float (&a)[24], const u32x4* A, const u32x4* B, const unsigned (&xp)[3]) {
#pragma unroll
  for (int jj = 0; jj < 3; ++jj)
#pragma unroll
    for (int i = 0; i < 4; ++i) {
      a[8 * jj + 2 * i] = dot2(plo(A[jj][i], B[jj][i]), xp[jj], a[8 * jj + 2 * i]);
      a[8 * jj + 2 * i + 1] = dot2(phi(A[jj][i], B[jj][i]), xp[jj], a[8 * jj + 2 * i + 1]);
    }
}
__device__ __forceinline__ f32x4 red_rows(const float (&a)[24], bool t1) {
  float bq[12], v[6];
#pragma unroll
  for (int k = 0; k < 12; ++k) bq[k] = sw32(a[k], a[k + 12]);
#pragma unroll
  for (int k = 0; k < 6; ++k) { v[k] = sw16(bq[k], bq[k + 6]); v[k] += dppf<ROR8>(v[k]); }
  return f32x4{t1 ? v[4] : v[0], t1 ? v[5] : v[1], v[2], v[3]};
}
template <int N, int STRIDE>
__device__ __forceinline__ unsigned poll_lane(rsrc_t rw, int voff, u32x4 (&v)[N], unsigned ta, unsigned tb) {
  unsigned spins = 0;
#pragma unroll 1
  for (; spins < SPIN_CAP; ++spins) {
    if (none(units_bad<N>(v, ta, tb) != 0u)) break;
#pragma unroll
    for (int k = 0; k < N; ++k) {
      const unsigned long long em = __builtin_amdgcn_ballot_w64(((v[k].y ^ ta) | (v[k].w ^ tb)) != 0u);
      asm volatile("s_mov_b64 exec, %3\n\tbuffer_load_dwordx4 %0, %1, %2, %4 offen sc1\n\ts_mov_b64 exec, -1" : "+v"(v[k]) : "v"(voff), "s"(rw), "s"(em), "s"(k * STRIDE) : "memory");
    }
    if constexpr (N == 2) asm volatile("s_waitcnt vmcnt(0)" : "+v"(v[0]), "+v"(v[1]) :: "memory");
    else asm volatile("s_waitcnt vmcnt(0)" : "+v"(v[0]), "+v"(v[1]), "+v"(v[2]), "+v"(v[3]) :: "memory");
  }
  return spins;
}
#define SL_ "s_sleep 2\n\t"
#define RING(p, t, em) asm volatile("s_mov_b64 exec, %2\n\tglobal_atomic_swap %0, %1, off sc1\n\ts_mov_b64 exec, -1" :: "v"(p), "v"(t), "s"(em) : "memory")
__device__ __forceinline__ void wait_marks(const void* p, unsigned tg, unsigned long long em, unsigned (&s)[4]) {
  unsigned cnt = SPIN_CAP / 4;
#define A1_(a) "global_atomic_add %[" #a "], %[p], %[z], off sc0 sc1\n\t"
#define CH_(a) "s_waitcnt vmcnt(3)\n\tv_cmpx_ne_u32 exec, %[" #a "], %[tg]\n\ts_cbranch_execz 2f\n\t" SL_
  asm volatile(
      "s_mov_b64 exec, %[em]\n\t"
      A1_(a) SL_ A1_(b) SL_ A1_(c) SL_
      "1:\n\t"
      A1_(d) CH_(a)
      A1_(a) CH_(b)
      A1_(b) CH_(c)
      A1_(c) CH_(d)
      "s_sub_u32 %[cnt], %[cnt], 1\n\t"
      "s_cmp_lg_u32 %[cnt], 0\n\t"
      "s_cbranch_scc1 1b\n\t"
      "2:\n\t"
      "s_mov_b64 exec, -1"
      : [cnt] "+s"(cnt), [a] "=&v"(s[0]), [b] "=&v"(s[1]), [c] "=&v"(s[2]), [d] "=&v"(s[3])
      : [p] "v"(p), [z] "v"(0u), [tg] "v"(tg), [em] "s"(em)
      : "memory", "vcc", "scc");
}
__device__ __forceinline__ void announce(const void* p, unsigned tg, unsigned t, unsigned long long em) {
  asm volatile("s_mov_b64 exec, %2\n\tglobal_atomic_swap %0, %1, off sc1\n\ts_mov_b64 exec, -1" :: "v"(p), "v"((tg << 12) | (t & 0xFFFu)), "s"(em) : "memory");
}
__device__ __forceinline__ unsigned gate(const void* p, unsigned tg, unsigned long long em, unsigned (&s)[4]) {
  unsigned cnt = SPIN_CAP / 4, res = 0u, t;
#define CI_(a) "s_waitcnt vmcnt(3)\n\tv_mov_b32 %[res], %[" #a "]\n\tv_lshrrev_b32 %[t], 12, %[" #a "]\n\tv_cmpx_ne_u32 exec, %[t], %[tg]\n\ts_cbranch_execz 2f\n\ts_sleep 1\n\t"
  asm volatile(
      "s_mov_b64 exec, %[em]\n\t"
      A1_(a) SL_ A1_(b) SL_ A1_(c) SL_
      "1:\n\t"
      A1_(d) CI_(a)
      A1_(a) CI_(b)
      A1_(b) CI_(c)
      A1_(c) CI_(d)
      "s_sub_u32 %[cnt], %[cnt], 1\n\t"
      "s_cmp_lg_u32 %[cnt], 0\n\t"
      "s_cbranch_scc1 1b\n\t"
      "2:\n\t"
      "s_mov_b64 exec, -1"
      : [cnt] "+s"(cnt), [res] "+&v"(res), [t] "=&v"(t), [a] "=&v"(s[0]), [b] "=&v"(s[1]), [c] "=&v"(s[2]), [d] "=&v"(s[3])
      : [p] "v"(p), [z] "v"(0u), [tg] "v"(tg & 0xFFFFFu), [em] "s"(em)
      : "memory", "vcc", "scc");
  const unsigned now = (unsigned)__builtin_amdgcn_s_memrealtime();
  const float age = max64(-(float)(res ? (now - res) & 0xFFFu : 0xFFFu));
  return now + (unsigned)__builtin_amdgcn_readfirstlane((int)age);
}
__device__ __forceinline__ void until(unsigned due) {
  while (due - (unsigned)__builtin_amdgcn_s_memrealtime() - 1u < 400u) {}
}
#define KEEP(s) asm volatile("" :: "v"(s[0]), "v"(s[1]), "v"(s[2]), "v"(s[3]) : "memory")
typedef short s16x4 __attribute__((ext_vector_type(4)));
typedef __attribute__((ext_vector_type(8))) __bf16 bf16x8;
typedef __attribute__((address_space(3))) s16x4* lds_s16x4;
__device__ __forceinline__ u32x4 frag(unsigned a, int off, int off_hi) {
  const s16x4 lo = __builtin_amdgcn_ds_read_tr16_b64_v4i16((lds_s16x4)(size_t)(a + off));
  const s16x4 hi = __builtin_amdgcn_ds_read_tr16_b64_v4i16((lds_s16x4)(size_t)(a + off_hi));
  const u32x2 l2 = __builtin_bit_cast(u32x2, lo), h2 = __builtin_bit_cast(u32x2, hi);
  return u32x4{l2.x, l2.y, h2.x, h2.y};
}
__device__ __forceinline__ f32x4 mma(u32x4 a, u32x4 b, f32x4 c) {
  return __builtin_amdgcn_mfma_f32_16x16x32_bf16(__builtin_bit_cast(bf16x8, a), __builtin_bit_cast(bf16x8, b), c, 0, 0, 0);
}
typedef const __attribute__((address_space(3))) u32x4* lds_cv4;
__device__ __forceinline__ unsigned lds_addr(const void* p) { return (unsigned)(size_t)(const __attribute__((address_space(3))) void*)p; }
__device__ __forceinline__ u32x4 lds_ld4(unsigned a, int off) { return *(lds_cv4)(size_t)(a + off); }
__device__ __forceinline__ void lds_st(unsigned a, unsigned v) { *(lds_u32)(size_t)a = v; }
#define PINV(x) asm volatile("" : "+v"(x))
#define WAITC(i, n) while (lds_ld(&ctl[i]) < (n)) __builtin_amdgcn_s_sleep(1); asm volatile("" ::: "memory")
#define WAITS(i, n) while (lds_ld(&ctl[i]) < (n)) __builtin_amdgcn_s_sleep(4); asm volatile("" ::: "memory")
#define SPINC(i, n) while (lds_ld(&ctl[i]) < (n)) {} asm volatile("" ::: "memory")

__device__ __forceinline__ void dma1(rsrc_t r, int voff, int soff, unsigned lds) {
  asm volatile("s_mov_b32 m0, %0\n\ts_nop 0\n\tbuffer_load_dwordx4 %1, %2, %3 offen nt lds" :: "s"(lds), "v"(voff), "s"(r), "s"(soff) : "memory");
}
__device__ __forceinline__ void dma3(rsrc_t r, int voff, int soff, unsigned lds) {
  asm volatile("s_mov_b32 m0, %0\n\ts_nop 0\n\tbuffer_load_dwordx4 %1, %2, %3 offen nt lds\n\t"
               "s_add_u32 m0, m0, 896\n\ts_nop 0\n\tbuffer_load_dwordx4 %1, %2, %3 offen offset:128 nt lds\n\t"
               "s_add_u32 m0, m0, 896\n\ts_nop 0\n\tbuffer_load_dwordx4 %1, %2, %3 offen offset:256 nt lds" :: "s"(lds), "v"(voff), "s"(r), "s"(soff) : "memory", "scc");
}
#define VMW(n) asm volatile("s_waitcnt vmcnt(" #n ")" ::: "memory")
#define BAR0() asm volatile("s_barrier" ::: "memory")

__device__ __forceinline__ void code_ahead(int i) {
  asm volatile("s_getpc_b64 s[88:89]\n1:\n\t"
               "s_add_u32 s88, s88, .Lkf_begin - 1b\n\ts_addc_u32 s89, s89, -1\n\ts_and_b32 s89, s89, 0xffff\n\t"
               "s_mov_b32 s90, 28672\n\ts_mov_b32 s91, 0x20000\n\t"
               "s_buffer_load_dword s92, s[88:91], %0 offset:0\n\ts_buffer_load_dword s92, s[88:91], %0 offset:64\n\t"
               "s_buffer_load_dword s92, s[88:91], %0 offset:128\n\ts_buffer_load_dword s92, s[88:91], %0 offset:192\n\t"
               "s_buffer_load_dword s92, s[88:91], %0 offset:256\n\ts_buffer_load_dword s92, s[88:91], %0 offset:320\n\t"
               "s_buffer_load_dword s92, s[88:91], %0 offset:384\n\ts_waitcnt lgkmcnt(0)"
               :: "s"(i * 448) : "memory", "scc", "s88", "s89", "s90", "s91", "s92");
}
__device__ __forceinline__ void survey(rsrc_t rw, int off, int slot, unsigned* cnt, unsigned c0, unsigned dc, unsigned t0, unsigned dt, unsigned ta, unsigned tb, int lane) {
#define NOW() (unsigned)__builtin_amdgcn_s_memrealtime()
#define PACE(due) { PIN(); const unsigned d_ = (due); while ((int)(NOW() - d_) < 0) __builtin_amdgcn_s_sleep(1); PIN(); }
  if (slot >= 0) {
    unsigned a = 0u;
    int l0 = 0, l1 = 0;
#pragma unroll 1
    for (unsigned i = 0; i < 5; ++i) {
      if (i < 4) while (lds_ld(cnt) < c0 + i * dc) __builtin_amdgcn_s_sleep(1);
      asm volatile("buffer_load_dword %0, %1, %2, 0 offen\n\ts_waitcnt vmcnt(1)" : "+v"(a) : "v"(OOB), "s"(rw) : "memory");
      const int l = min((int)(NOW() - t0 - (i - 1) * dt / 4), 130);
      if (i >= 3) l1 += l; else if (i) l0 += l;
    }
    put_unit(rw, lane < 8 ? off + slot * 128 + lane * 16 : OOB, 0.5f * (float)l0, 0.5f * (float)l1, ta, tb);
    asm volatile("s_waitcnt vmcnt(1)" :: "v"(a) : "memory");
  }
}
__device__ __forceinline__ f32x4 result(rsrc_t rw, int off, unsigned t2, unsigned ta, unsigned tb, int lane) {
  PACE(t2)
  u32x4 v[1];
  load_units<1, 0>(rw, off + (lane & 31) * 128, v);
  const unsigned u0 = v[0].x, u1 = v[0].z;
  const bool ok = units_bad<1>(v, ta, tb) == 0u, in = ok && ((lane & 31) >= 21) == (lane >= 32);
  const float x = red32(ok ? __uint_as_float(lane < 32 ? u0 : u1) : 0.f), n = red32(ok ? 1.f : 0.f);
  const float m = n < 16.f ? 1000.f : x * __builtin_amdgcn_rcpf(n);
  const float g = red32(in ? __uint_as_float(u0) + __uint_as_float(u1) : 0.f) * __builtin_amdgcn_rcpf(fmaxf(red32(in ? 2.f : 0.f), 1.f));
#define RL_(x, l) __uint_as_float(__builtin_amdgcn_readlane(__float_as_uint(x), l))
  return f32x4{RL_(m, 0), RL_(m, 32), RL_(g, 32) - RL_(g, 0), 0.f};
}
__global__ __launch_bounds__(NTHR, 4) void k_fused(const u16* x_, const u16* kc_, const u16* wq_, unsigned char* ws_, const u16* vc_, const u16* wo_,
                                                   const u16* w1_, const u16* w3_, const u16* w2_, u16* out_) {
  typedef unsigned u32x8 __attribute__((ext_vector_type(8)));
  typedef __attribute__((address_space(1))) unsigned char* gp_t;
  const unsigned long long kap = (unsigned long long)__builtin_amdgcn_kernarg_segment_ptr();
  u32x8 ka, kb;
  u32x4 kc4;
  asm volatile(".Lkf_begin:\n\ts_load_dword s95, %0, 0x0" :: "s"(kap) : "memory", "s95");
#define ARGS() asm volatile("s_load_dwordx8 %0, %1, 0x0\n\ts_waitcnt lgkmcnt(0)" : "=&s"(ka) : "s"(kap) : "memory", "s95")
#define ARGS2() asm volatile("s_load_dwordx8 %0, %2, 0x20\n\ts_load_dwordx4 %1, %2, 0x40\n\ts_waitcnt lgkmcnt(0)" : "=&s"(kb), "=&s"(kc4) : "s"(kap) : "memory")
#define ARG(v, i) ((gp_t)(((unsigned long long)v[2 * (i) + 1] << 32) | v[2 * (i)]))
  __shared__ __attribute__((aligned(1024))) unsigned char smem[L_END];
  float* const xs = reinterpret_cast<float*>(smem + L_XS);
  unsigned* const xraw = reinterpret_cast<unsigned*>(smem + L_XRAW);
  f32x4* const part = reinterpret_cast<f32x4*>(smem + L_PART);
  unsigned* const qs = reinterpret_cast<unsigned*>(smem + L_QS);
  unsigned* const hs = reinterpret_cast<unsigned*>(smem + L_HS);
  float* const wmax = reinterpret_cast<float*>(smem + L_WMAX);
  float* const lsum = reinterpret_cast<float*>(smem + L_LSUM);
  unsigned* const ctl = reinterpret_cast<unsigned*>(smem + L_CTL);
  const unsigned lb = lds_addr(smem);
  __builtin_amdgcn_s_setprio(3);
  const float L2E = 1.4426950408889634f;
  const int tid = threadIdx.x, lane = tid & 63, r8 = lane >> 3, c8 = lane & 7;
  int w = __builtin_amdgcn_readfirstlane(tid >> 6);
  const int b = blockIdx.x, xcd = b & 7, j = b >> 3, h = xcd + 8 * (j >> 3), s = j & 7;
  const bool lt = xcd & 1;
  const bool one = lt || j < 16;
#define BLK(i) ((h * SK + (SK - WIN) + ((i) * 8 + s) * 32) * (DH * 2))
#define LDK() { const rsrc_t rk = mk((const void*)ARG(ka, 1), H * SK * DH * 2); \
    _Pragma("unroll") for (int g = 0; g < 4; ++g) _Pragma("unroll") for (int jj = 0; jj < 3; ++jj) \
    K[3 * g + jj] = __builtin_amdgcn_raw_buffer_load_b128(rk, vkv + jj * 128, sb + g * 3072, NT); }
#define SHIFT(hd, r, t) (((int)(((hd) * (r) / 32u + (r) - (((t) & 0xFFFFFFFu) * 4u) % (r)) % (r)) - (int)(r) / 2) / 4)
#define DUE(n) (tz + (n) * pd / 4)
#define ORG(t) while ((t = lds_ld(&ctl[30])) == 0u) __builtin_amdgcn_s_sleep(1)
#define KPACE(n) { ORG(tz); tz += SHIFT(h, (unsigned)pd, tz) + P0; PACE(DUE(n) - 8) PACE(DUE(n) + lds_ld(&ctl[6]) + (n) * lds_ld(&ctl[7]) / 4) }
  unsigned ta = 0u, tb = 0u;
  unsigned tz;
  const int pw = (w + 14) & 15, vkv = r8 * 384 + c8 * 16, sb = BLK(pw);
  const bool t1 = (lane >> 3) & 1;
#define PAIRS() const int k0 = 6 * (lane >> 4) + ((lane >> 1) & 4), k2 = (lane & 8) ? k0 : k0 + 2; \
  const int pr0 = 32 * (k0 >> 3) + 4 * c8 + ((k0 & 7) >> 1), pr1 = 32 * (k2 >> 3) + 4 * c8 + ((k2 & 7) >> 1)
  if (tid < 32) ctl[tid] = 0u;
  int sq[3], sg = w < 2 ? 8 * (H * DH * 2) : 3072, vq = r8 * (sg >> 3) + c8 * 16;
#pragma unroll
  for (int jj = 0; jj < 3; ++jj) {
    const int c = 6 * xcd + 3 * w + jj;
    int t = (c & 15) * 40 * (H * DH * 2) + j * (DH * 2) + (c >> 4) * 128;
    asm volatile("" : "+s"(t));
    sq[jj] = w < 2 ? t : sb + jj * 128;
    asm volatile("" : "+s"(sq[jj]));
  }
  asm volatile("" : "+s"(sg));
  int vq4 = w < 2 ? vq : OOB, vx = w < 2 ? lane * 4 : OOB;
  PINV(vq); PINV(vq4); PINV(vx);
  unsigned xr[5];
  u32x4 W[15];
#define K W
  if (w >= 2) __builtin_amdgcn_s_setprio(2);
  PIN();
  ARGS();
  asm volatile("s_cmp_lt_u32 %0, 2\n\ts_cbranch_scc1 1f\n\ts_barrier\n\t1:" : "+s"(w) :: "scc", "memory");
  if (w < 3) {
    const rsrc_t rx = mk((const void*)ARG(ka, 0), D * 2), rq = mk((const void*)(w < 2 ? ARG(ka, 2) : ARG(ka, 1)), w < 2 ? D * H * DH * 2 : H * SK * DH * 2);
#pragma unroll
    for (int k = 0; k < 5; ++k) xr[k] = __builtin_amdgcn_raw_buffer_load_b32(rx, vx + k * 256, 0, 0);
    PIN();
#pragma unroll
    for (int g = 0; g < 5; ++g) {
#pragma unroll
      for (int jj = 0; jj < 3; ++jj)
        W[3 * g + jj] = __builtin_amdgcn_raw_buffer_load_b128(rq, g < 4 ? vq : vq4, sq[jj] + g * sg, NT);
      PIN();
    }
  }
  asm volatile("s_cmp_ge_u32 %0, 2\n\ts_cbranch_scc1 1f\n\ts_barrier\n\t1:" : "+s"(w) :: "scc", "memory");
  if (w == 0) { ctl[30] = NOW() | 1u; }
  unsigned long long eps;
  asm volatile("s_cmp_lg_u32 %2, 12\n\ts_cbranch_scc1 1f\n\ts_load_dwordx2 %0, %1, 0x0\n\ts_waitcnt lgkmcnt(0)\n\t1:" : "=&s"(eps) : "s"(ARG(ka, 3)), "s"(w) : "memory", "scc");
  if (w >= 14) code_ahead(2 * j + (w & 1));
  if (w == 10 && (j & 1) && j < 30) {
    const int i = j >> 1, ai = i < 4 ? 0 : i < 9 ? 1 : (i - 5) >> 1;
    unsigned o = i < 4 ? ((xcd + 8 * i) * SK + SK - 1) * (DH * 2) : (i < 9 ? i - 4 : i & 1) * (D * F * 2 - 4096) + xcd * 512;
    asm volatile("s_load_dwordx2 s[88:89], %1, %2\n\ts_waitcnt lgkmcnt(0)\n\tglobal_load_dword %0, %0, s[88:89] nt\n\ts_waitcnt vmcnt(0)" : "+v"(o) : "s"(kap), "s"(32 + 8 * ai) : "memory", "s88", "s89");
  }
  if (w > 2 && pw < QM) {
    const int pd = RK;
    KPACE(pw)
    LDK()
    PIN();
    lds_inc(&ctl[4], lane);
#define SLOT() (s == ((h * 6) & 7) ? h : -1)
    if ((pw >> 1) == 1 && SLOT() >= 0) {
      const unsigned t0 = NOW();
      VMW(0);
      const unsigned l = min(NOW() - t0, 120u);
      if (pw == 2) ctl[14] = l;
      else {
        WAITC(5, 1u);
        put_unit(mk((unsigned char*)ARG(ka, 3), WS_END), lane < 8 ? WS_LB + h * 128 + lane * 16 : OOB, (float)lds_ld(&ctl[14]), (float)l, lds_ld(&ctl[0]), lds_ld(&ctl[1]));
      }
    }
  }
  asm volatile("" : "+s"(w));
  if (w < 2) {
    unsigned char* ws = (unsigned char*)ARG(ka, 3);
    const rsrc_t rw = mk(ws, WS_END);
    __builtin_amdgcn_s_setprio(0);
    PAIRS();
    if (w == 1 && lane < 56) hs[40 + lane] = (lane >= 8 && lane < 48) ? 0x3F803F80u : 0u;
    PIN();
    {
      float sm = 0.f;
#pragma unroll
      for (int k = 0; k < 5; ++k) sm += lo16(xr[k]) + hi16(xr[k]);
      const float mean = red64(sm) * (1.f / D);
      float q2 = 0.f;
#pragma unroll
      for (int k = 0; k < 5; ++k) { const float a = lo16(xr[k]) - mean, bb = hi16(xr[k]) - mean; q2 += a * a + bb * bb; }
      const float var = bfr(red64(q2) * (1.f / D));
      const float mb = bfr(mean), den = bfr(sqrtf(bfr(var + 1e-5f)));
#pragma unroll
      for (int k = 0; k < 5; ++k) {
        f32x2 v = {bfr(bfr(lo16(xr[k]) - mb) / den), bfr(bfr(hi16(xr[k]) - mb) / den)};
        *reinterpret_cast<f32x2*>(&xs[2 * (lane + 64 * k)]) = v;
        xraw[lane + 64 * k] = xr[k];
      }
    }
    __builtin_amdgcn_fence(__ATOMIC_RELEASE, "wavefront");
    __builtin_amdgcn_wave_barrier();
    {
      unsigned x01[3], x23[3], x4[3];
#pragma unroll
      for (int jj = 0; jj < 3; ++jj) {
        const float* xp = &xs[((6 * xcd + 3 * w + jj) & 15) * 40 + r8];
        x01[jj] = pk2(xp[0], xp[8]); x23[jj] = pk2(xp[16], xp[24]); x4[jj] = pk2(xp[32], 0.f);
      }
      float a[24];
#pragma unroll
      for (int k = 0; k < 24; ++k) a[k] = 0.f;
      pv_pair(a, &W[0], &W[3], x01);
      pv_pair(a, &W[6], &W[9], x23);
      pv_pair(a, &W[12], &W[12], x4);
      const f32x4 r4 = red_rows(a, t1);
      f32x2* const pp2 = reinterpret_cast<f32x2*>(part + 48 * w);
      if (!t1) pp2[pr1] = f32x2{r4.z, r4.w};
      pp2[pr0] = f32x2{r4.x, r4.y};
    }
    lds_inc(&ctl[2], lane);
    if (!w) {
      WAITC(5, 1u);
      ta = lds_ld(&ctl[0]); tb = lds_ld(&ctl[1]);
      while (lds_ld(&ctl[2]) != 2u) {}
      asm volatile("" ::: "memory");
      const int cl = (6 * xcd >> 4) + (lane >> 5);
      f32x2 a2 = {0.f, 0.f};
#pragma unroll
      for (int i = 0; i < 6; ++i) { const f32x2 t = reinterpret_cast<const f32x2*>(part)[32 * i + (lane & 31)]; if (((6 * xcd + i) >> 4) == cl) a2 += t; }
      int qo = cl <= ((6 * xcd + 5) >> 4) ? WS_PQ + j * 5120 + (xcd + (xcd > 2) + (xcd > 5)) * 512 + lane * 16 : OOB;
      PINV(qo); PINV(a2);
      put_unit<SC1 | NT>(rw, qo, a2.x, a2.y, ta, tb);
    }
    __builtin_amdgcn_s_setprio(2);
  }
  asm volatile("" : "+s"(w));
  if (w < 2 || w >= QM + 2) {
    const int pd = RK;
    if (w == 13 && j == ((5 * xcd + 2) & 7) * 4 + (xcd & 3)) {
      const unsigned long long pr = (unsigned long long)ARG(ka, 3) + WS_END;
      asm volatile("s_load_dword s88, %0, 0x0\n\ts_load_dword s89, %0, 0x2000\n\ts_load_dword s90, %0, 0x5000\n\ts_load_dword s91, %0, 0x7000\n\ts_waitcnt lgkmcnt(0)"
                   :: "s"(pr + ((xcd + 1) & 7) * 256) : "memory", "s88", "s89", "s90", "s91");
      unsigned t0;
      ORG(t0);
      PACE(t0 + PT)
      int d = 0;
#pragma unroll 1
      for (int i = 0; i < 4; ++i) {
        unsigned long long c0, c1;
        asm volatile("s_memtime %0\n\ts_waitcnt lgkmcnt(0)\n\ts_load_dword s88, %2, %3\n\ts_waitcnt lgkmcnt(0)\n\ts_memtime %1\n\ts_waitcnt lgkmcnt(0)"
                     : "=&s"(c0), "=&s"(c1) : "s"(pr + xcd * 256), "s"((i & 1) * 0x2000 + (i >> 1) * 0x5000) : "memory", "s88");
        const int t = min((int)(c1 - c0), 1200);
        d = -d + t;
      }
      WAITC(5, 1u);
      put_unit(mk((unsigned char*)ARG(ka, 3), WS_END), lane < 8 ? WS_TO + xcd * 128 + lane * 16 : OOB, __uint_as_float(t0), (float)(xcd < 4 ? -d : d), lds_ld(&ctl[0]), lds_ld(&ctl[1]));
    }
#define JUDGE(m, th, sl, lv) const int sl = ((m.y - m.x > th && m.y > 86.5f) || m.x > 500.f) ? 1 : 0, lv = min(max((int)m.x - LBT, 0) * LB0 / 8, 40)
    if (w == 12) {
      eps += 1ull;
      ta = (unsigned)eps; tb = (unsigned)(eps >> 32) ^ 0x9E3779B9u;
      if (lane == 0) { ctl[0] = ta; ctl[1] = tb; }
      lds_inc(&ctl[5], lane);
      unsigned t0;
      ORG(t0);
      const f32x4 m = result(mk((unsigned char*)ARG(ka, 3), WS_END), WS_LB, t0 + P0 + 3 * pd / 4 + SRD, ta, tb, lane);
      const int sl = (m.y - m.x + m.z + fminf(fmaxf(0.4f * (m.x - 66.f), 0.f), 2.5f) > 0.5f * (float)THK || m.x > 500.f) ? 1 : 0, lv = min(max((int)m.x - LBK, 0) * LB0 / 8, 40);
      ctl[6] = lv; ctl[7] = sl * SLK; ctl[25] = lv + 4 * sl * SLK;
    }
    KPACE(w < 2 ? QM + w : w)
    LDK()
    PIN();
    lds_inc(&ctl[4], lane);
  }
#define F_(x) __uint_as_float(x)
  ARGS2();
  asm volatile("" : "+s"(w));
  unsigned char* ws = (unsigned char*)ARG(ka, 3);
  const u16* wo = (const u16*)ARG(kb, 1); const u16* w1 = (const u16*)ARG(kb, 2); const u16* w3 = (const u16*)ARG(kb, 3);
  const u16* w2 = (const u16*)ARG(kc4, 0); u16* out = (u16*)ARG(kc4, 1);
  const rsrc_t rl = mk(ws, WS_END);
  const int pd = one ? RV : RV2;
  const int dw = w & 1, csw = (c8 ^ (r8 & 2) ^ (4 * dw)) * 16;
  {
    if (w == 0) {
      __builtin_amdgcn_s_setprio(3);
      u32x4 qv[5], ov[1];
      int qo = WS_PQ + h * 5120 + lane * 16, oo = WS_TO + c8 * 128;
#pragma unroll 1
      for (unsigned spins = 0; spins < SPIN_CAP; ++spins) {
        asm volatile("" : "+v"(qo), "+v"(oo) :: "memory");
        load_units<5, 1024, SC1 | NT>(rl, qo, qv);
        load_units<1, 0>(rl, oo, ov);
        if (none(units_bad<1>(ov, ta, tb, units_bad<5>(qv, ta, tb)) != 0u)) break;
      }
      f32x2 v[5];
#pragma unroll
      for (int k = 0; k < 5; ++k) { const unsigned u0 = qv[k].x, u1 = qv[k].z; v[k] = f32x2{__uint_as_float(u0), __uint_as_float(u1)}; }
      const bool hi = lane >= 32;
      const f32x2 c0 = hi ? v[0] : v[0] + v[1], c1 = hi ? v[1] + v[2] : v[2] + v[3], c2 = hi ? v[3] + v[4] : v[4];
      qs[lane] = pk2(sw32(c0.x, c1.x), sw32(c0.y, c1.y));
      qs[64 + (lane & 31)] = pk2(sw32(c2.x, c2.x), sw32(c2.y, c2.y));
      const unsigned o_ = ov[0].x, o0 = __builtin_amdgcn_readfirstlane(o_);
      float od = (float)(int)(o_ - o0);
      od += dppf<XOR1>(od); od += dppf<XOR2>(od); od += dppf<HMIR>(od);
      const unsigned p_ = ov[0].z;
      float vt = __uint_as_float(p_);
      vt += dppf<XOR1>(vt); vt += dppf<XOR2>(vt); vt += dppf<HMIR>(vt);
      asm volatile("s_waitcnt lgkmcnt(0)" ::: "memory");
      ctl[3] = ((o0 + (unsigned)(int)(od * 0.125f)) & ~3u) | (vt > 0.f ? 3u : 1u);
    }
  }
  __builtin_amdgcn_s_setprio(1);
  __builtin_amdgcn_s_waitcnt(0x0F70);
  asm volatile("" :: "v"(W[12]), "v"(W[13]), "v"(W[14]), "v"(xr[0]), "v"(xr[1]), "v"(xr[2]), "v"(xr[3]), "v"(xr[4]));
  unsigned tc;
  int pol;
  float wm;
  {
    const rsrc_t rv = mk((const void*)ARG(kb, 0), H * SK * DH * 2);
    const int vv = r8 * 384 + csw;
#define SH(n) (lds_ld(&ctl[25]) + (n) * lds_ld(&ctl[8]) * (one ? 8 : 7) / 32)
#define SHC() (lds_ld(&ctl[25]) + 15 * lds_ld(&ctl[8]) / 4)
#define LDV(vg) { WAITC(4, NK + 8 * (vg)); \
    int so_ = BLK(4 * (vg)) + dw * 3072; unsigned ld_ = lb + L_RING + ((vg) == 3 ? 0 : (vg)) * 49152 + dw * 3072; \
    _Pragma("unroll 1") for (int i = 4 * (vg); i < 4 * (vg) + 4; ++i) { PACE(DUE(i) - 8) PACE(DUE(i) + SH(i)) \
      dma3(rv, vv, so_, ld_); dma3(rv, vv, so_ + 6144, ld_ + 6144); so_ += 98304; ld_ += 12288; lds_inc(&ctl[4], lane); } \
    }
    unsigned tg;
    while ((tg = lds_ld(&ctl[3])) == 0u) __builtin_amdgcn_s_sleep(2);
    asm volatile("" ::: "memory");
    pol = (tg & 2u) << 12;
    const unsigned to = lds_ld(&ctl[30]);
    tg = to + min(max((int)(tg - to), -300), 300);
    tz = tg + SHIFT(h, (unsigned)RK, tg) + P0 + GK + 4 * RK;
    tc = tg + P0 + GK + 4 * RK + 15 * RV / 4;
    ta = __builtin_amdgcn_readfirstlane(ctl[0]); tb = __builtin_amdgcn_readfirstlane(ctl[1]);
    float pe;
    {
      const unsigned* qp = &qs[4 * c8];
      u32x4 qf[3];
#pragma unroll
      for (int jj = 0; jj < 3; ++jj) qf[jj] = *reinterpret_cast<const u32x4*>(qp + 32 * jj);
      float d[4];
#pragma unroll
      for (int g = 0; g < 4; ++g) {
        d[g] = 0.f;
#pragma unroll
        for (int jj = 0; jj < 3; ++jj)
#pragma unroll
          for (int i = 0; i < 4; ++i) d[g] = dot2(K[3 * g + jj][i], qf[jj][i], d[g]);
        d[g] += dppf<HMIR>(d[g]);
      }
      const float e0 = rs<XOR2>(d[0], d[2], lane & 2), e1 = rs<XOR2>(d[1], d[3], lane & 2);
      float sc = rs<XOR1>(e0, e1, lane & 1);
      sc = bfr(bfr(sc) * 0.07216878364870322f);
      wm = max64(sc);
      pe = sc;
      if (lane == 0) wmax[w] = wm;
      lds_inc(&ctl[9], lane);
    }
    const int vg = (0x42144403 >> (4 * (w >> 1))) & 15;
    if (vg < 2) LDV(vg)
    if (w == 9) survey(rl, WS_LB + 4096, SLOT(), &ctl[4], NK + 2u, 2u, DUE(0) + lds_ld(&ctl[25]), pd, ta, tb, lane);
    while (lds_ld(&ctl[9]) < NST) __builtin_amdgcn_s_sleep(2);
    asm volatile("" ::: "memory");
    u16* const pp = reinterpret_cast<u16*>(smem + L_P) + 32 * pw + 8 * (lane & 3) + r8;
    wm = max16(wmax[lane & 15]);
    pe = __builtin_amdgcn_exp2f((pe - wm) * L2E);
    {
      const float p = bfr(pe);
      *pp = (u16)(__float_as_uint(p) >> 16);
      const float ls = red64(p) * 0.5f;
      if (lane == 0) lsum[w] = ls;
    }
    lds_inc(&ctl[10], lane);
    if (w >= 2) lds_inc(&ctl[16 + (pw >> 2)], lane);
    if (vg == 3) {
      WAITC(10, NST);
      *pp = (u16)pk2(pe * __builtin_amdgcn_rcpf(red16(lsum[lane & 15])), 0.f);
      WAITC(20, 6u);
    }
    if (vg < 4) {
      if (vg >= 2) LDV(vg)
      if (vg == 3) VMW(12); else VMW(0);
      lds_inc(&ctl[16 + vg], lane);
    }
  }
  const bool role4 = one, pvw = w >= 4 && w < 10;
  const int q3 = j & 3;
  const int ho = ((!role4 && q3 == 3) ? xcd : xcd ^ 1) + 8 * (lt ? j >> 3 : (j >> 2) & 3), ob = lt ? j & 7 : role4 ? q3 : (q3 == 3 ? 8 : 4 + 2 * q3);
  const int rg = j < 24 ? xcd : xcd ^ 1, ln = j < 24 ? j : j - 8, n5 = 12 * (xcd >> 1) + j - 16;
  const int g4 = lane >> 4, tq = (lane >> 2) & 3, tp = lane & 3;
#define FRAG(t) (g4 * 1024 + tq * 128 + (((2 * (t) + (tp >> 1)) ^ (tq & 2) ^ (4 * (g4 & 1))) * 16) + (tp & 1) * 8)
  const int vo = r8 * (D * 2) + csw;
  const unsigned n12 = (role4 ? 4u : 8u);
  if (pvw) {
    const int t2 = w - 4;
    unsigned va = lb + L_RING + 2 * g4 * 1024 + (t2 >> 1) * 1024 + FRAG(2 * (t2 & 1)), vb = va ^ 32u, pa = lb + L_P + 16 * g4;
    f32x4 ac0 = {0.f, 0.f, 0.f, 0.f}, ac1 = {0.f, 0.f, 0.f, 0.f};
#define PVB(i) { const u32x4 pf = lds_ld4(pa, 64 * (i)); \
    const u32x4 a0 = frag(va, ((i) & 3) * 12288, ((i) & 3) * 12288 + 512), a1 = frag(vb, ((i) & 3) * 12288, ((i) & 3) * 12288 + 512); \
    ac0 = mma(a0, pf, ac0); ac1 = mma(a1, pf, ac1); }
#define PVD() PINV(ac0); PINV(ac1); PIN();
    WAITC(16, 6u);
    PVB(0) PVB(1) PVB(2) PVB(3) va += 49152; vb += 49152;
    lds_inc(&ctl[20], lane);
    PVD()
    WAITC(17, 6u);
    PVB(4) PVB(5) PVB(6) PVB(7) va += 49152; vb += 49152;
    lds_inc(&ctl[21], lane);
    PVD()
    WAITC(10, NST);
    float rl_ = __builtin_amdgcn_rcpf(red16(lsum[lane & 15]));
    PINV(rl_);
    WAITC(18, 6u);
    PVB(8) PVB(9) PVB(10) PVB(11) va -= 98304; vb -= 98304;
    lds_inc(&ctl[22], lane);
    PVD()
    __builtin_amdgcn_s_setprio(3);
    SPINC(19, 4u);
    PVB(12) PVB(13)
    ac0 *= rl_; ac1 *= rl_;
    u32x4 pf = lds_ld4(pa, 64 * 14), pg = lds_ld4(pa, 64 * 15);
    int p1off = (lane & 14) ? OOB : (at_rec(h) ^ pol) + s * 1024 + (8 * t2 + 4 * (lane & 1) + g4) * 16;
    PINV(ac0); PINV(ac1); PINV(pf); PINV(pg); PINV(p1off); PINV(va); PINV(vb);
    SPINC(23, 2u);
    const u32x4 a0 = frag(va, 2 * 12288, 2 * 12288 + 512), a1 = frag(vb, 2 * 12288, 2 * 12288 + 512);
    const u32x4 a2 = frag(va, 3 * 12288, 3 * 12288 + 512), a3 = frag(vb, 3 * 12288, 3 * 12288 + 512);
    PIN();
    ac0 = mma(a0, pf, ac0); ac1 = mma(a1, pf, ac1);
    ac0 = mma(a2, pg, ac0); ac1 = mma(a3, pg, ac1);
    const unsigned x0 = pk2(ac0.x, ac0.y), z0 = pk2(ac0.z, ac0.w), x1 = pk2(ac1.x, ac1.y), z1 = pk2(ac1.z, ac1.w);
    put_unit(rl, p1off, __uint_as_float((lane & 1) ? x1 : x0), __uint_as_float((lane & 1) ? z1 : z0), ta, tb);
  } else if (w < 2) {
    __builtin_amdgcn_s_setprio(3);
    VMW(0);
    lds_inc(&ctl[23], lane);
    tc += SHC();
    const int lag = (int)((unsigned)__builtin_amdgcn_s_memrealtime() - tc) - TY;
    if (!w) ctl[26] = ((tc + (lag > 0 ? lag : 0)) | 1u);
    BAR();
  } else if (w < 4) {
    const rsrc_t ro = mk(wo, H * DH * D * 2);
    const int so_o = ((ho * DH + 8 * dw) * D + 64 * ob) * 2;
    WAITS(21, 6u);
    WAITS(4, NK + 32);
    const unsigned t0 = role4 ? tc + TW + SHC() : DUE(16) + SH(16), dt = role4 ? WQD : pd / 8;
    __builtin_amdgcn_s_setprio(3);
#pragma unroll 1
    for (int q = 0; q < (role4 ? 4 : 8); ++q) {
      until(t0 + q * dt);
#pragma unroll
      for (int n = 0; n < 3; ++n)
        dma1(ro, vo, so_o + (q >> 2) * 128 + ((q & 3) * 3 + n) * 16 * D * 2, lb + L_WO + (2 * (3 * q + n) + dw) * 1024);
    }
    VMW(3);
    lds_inc(&ctl[28], lane);
    VMW(0);
    BAR0();
  } else if (w == 14) {
    const unsigned s0 = lds_ld(&ctl[25]);
    const f32x4 m = result(rl, WS_LB + 4096, tc - 3 * RV + s0 + SRV, ta, tb, lane);
    JUDGE(m, 0.5f * (float)THV, sl, lv);
    ctl[25] = s0 + lv; ctl[8] = sl * SLV;
  } else if (w == 11) {
    WAITS(10, NST);
    const float lse = fmaf(wm, L2E, __builtin_amdgcn_logf(red16(lsum[lane & 15])));
    WAITS(18, 6u);
    tc += SHC();
    PACE(tc)
    put_unit(rl, lane < 8 ? (at_rec(h) ^ pol) + s * 1024 + 768 + lane * 16 : OOB, lse, lse, ta, tb);
    PACE(tc + TL)
    u32x4 lv_[1];
    poll_units<1, 0>(rl, (at_rec(ho) ^ pol) + c8 * 1024 + 768, lv_, ta, tb);
    const unsigned mu = lv_[0].x;
    const float ms = __uint_as_float(mu);
    float Mg = fmaxf(ms, dppf<XOR1>(ms)); Mg = fmaxf(Mg, dppf<XOR2>(Mg)); Mg = fmaxf(Mg, dppf<HMIR>(Mg));
    const float wg = __builtin_amdgcn_exp2f(ms - Mg);
    float Lg = wg + dppf<XOR1>(wg); Lg += dppf<XOR2>(Lg); Lg += dppf<HMIR>(Lg);
    lds_st(lb + L_WRE + 4 * c8, __float_as_uint(wg * __builtin_amdgcn_rcpf(Lg)));
    lds_inc(&ctl[28], lane);
  }
  __builtin_amdgcn_s_setprio(3);
  if (w >= (role4 ? 12 : 8)) {
    const int bk = w < 12;
    unsigned ba = lb + L_WO + bk * 24576 + FRAG(w & 3);
    unsigned ao = lb + L_REC + c8 * 416 + 16 * g4;
    int so = lane < 8 ? WS_P2 + (at_row(ho, 4 * (ob + bk) + (w & 3)) ^ pol) + lane * 16 : OOB;
    int ro_ = lane < 48 ? (at_rec(ho) ^ pol) + (w & 3) * 2048 + lane * 16 : OOB;
    unsigned ra = lb + L_REC + (w & 3) * 832 + (lane < 48 ? lane * 8 : 384);
    unsigned tl = lane < 48 ? ta : 0u, th = lane < 48 ? tb : 0u;
    PINV(ba); PINV(ao); PINV(so); PINV(ro_); PINV(ra); PINV(tl); PINV(th);
    u32x4 bf[6], af[6], v[2], wu;
    if (!bk) {
      WAITS(26, 1u);
      until(lds_ld(&ctl[26]) + TB);
      asm volatile("buffer_load_dwordx4 %0, %2, %3, 0 offen sc1\n\tbuffer_load_dwordx4 %1, %2, %3, 0 offen offset:1024 sc1" : "=&v"(v[0]), "=&v"(v[1]) : "v"(ro_), "s"(rl) : "memory");
#define AHEAD() SPINC(28, 3u);                 \
      _Pragma("unroll") for (int ks = 0; ks < 4; ++ks) bf[ks] = frag(ba, ks * 4096, ks * 4096 + 512); \
      wu = lds_ld4(lb + L_WRE + 16 * (g4 & 1), 0); \
      _Pragma("unroll") for (int ks = 0; ks < 4; ++ks) PINV(bf[ks]); \
      PINV(wu); PIN();
      AHEAD()
      asm volatile("s_waitcnt vmcnt(0)" : "+v"(v[0]), "+v"(v[1]) :: "memory");
#define RECS() _Pragma("unroll") for (int k = 0; k < 2; ++k) *(__attribute__((address_space(3))) u32x2*)(size_t)(ra + k * 416) = u32x2{v[k].x, v[k].z};
      RECS()
      if (RARE(!none(units_bad<2>(v, tl, th) != 0u))) {
        poll_lane<2, 1024>(rl, ro_, v, tl, th);
        RECS()
      }
    } else {
      WAITS(28, 1u);
      AHEAD()
    }
    BAR();
    const f32x4 wv = __builtin_bit_cast(f32x4, wu);
#pragma unroll
    for (int ks = 0; ks < 4; ++ks) af[ks] = lds_ld4(ao, 64 * ks);
#pragma unroll
    for (int ks = 4; ks < 6; ++ks) { af[ks] = lds_ld4(ao, 64 * ks); bf[ks] = frag(ba, ks * 4096, ks * 4096 + 512); }
    PIN();
    f32x4 acc = {0.f, 0.f, 0.f, 0.f}, ac2 = {0.f, 0.f, 0.f, 0.f};
#pragma unroll
    for (int ks = 0; ks < 6; ks += 2) { acc = mma(af[ks], bf[ks], acc); ac2 = mma(af[ks + 1], bf[ks + 1], ac2); }
    float c0 = fmaf(wv.w, acc.w, fmaf(wv.z, acc.z, fmaf(wv.y, acc.y, wv.x * acc.x)));
    const float c2 = fmaf(wv.y, ac2.y, wv.x * ac2.x), c3 = fmaf(wv.w, ac2.w, fmaf(wv.z, ac2.z, c0));
    c0 = c2 + c3;
    c0 = sw16(c0, c0);
    const float c1 = dppf<ROR8>(c0);
    put_unit(rl, so, c0, c1, ta, tb);
    PIN();
    lds_inc(&ctl[12], lane);
  }
  if (role4) {
    if ((0x0E0C >> w) & 1) {
      if (w >= 4) BAR0();
      const int hw = w < 4 ? w - 2 : w - 7;
      const int cl = 16 * hw + c8 + 8 * (lane >> 5);
      float xv = lo16(reinterpret_cast<const u16*>(xraw)[80 * rg + cl]);
      unsigned hoa = lb + L_HS + 2 * cl;
      u32x4 v[4];
      int po = WS_P2 + (at_row(r8, 5 * rg + hw) ^ pol) + c8 * 16;
      PINV(xv); PINV(hoa); PINV(po);
      const unsigned th_ = (lds_ld(&ctl[26]) + 100 + TD);
      __builtin_amdgcn_s_sleep(2);
      until(th_);
      load_units<4, 65536>(rl, po, v);
      poll_lane<4, 65536>(rl, po, v, ta, tb);
      float s0 = (F_(v[0].x) + F_(v[1].x)) + (F_(v[2].x) + F_(v[3].x));
      const float s1 = (F_(v[0].z) + F_(v[1].z)) + (F_(v[2].z) + F_(v[3].z));
      s0 = sw32(s0, s1);
      s0 = sw16(s0, s0);
      s0 += dppf<ROR8>(s0);
      s0 = bfr(s0) + xv;
      *(__attribute__((address_space(3))) u16*)(size_t)hoa = (u16)pk2(s0, s0);
      BAR();
    } else if (w < 13) {
      if (w >= 4 && w < 9) {
        const rsrc_t r1 = mk(w1, D * F * 2), r3 = mk(w3, D * F * 2);
        const int q = w - 4;
        const int v0 = r8 * (F * 2) + csw, v1 = v0 ^ 64, so = ((80 * rg + 8 * q) * F + ln * 64) * 2;
        const unsigned fd = lb + L_FF + q * 1024;
        WAITC(22, 6u);
        WAITS(26, 1u);
        until((lds_ld(&ctl[26]) + TB + F1D + (q >= NE ? F1S : 0)));
        dma1(r1, v0, so, fd); dma1(r3, v0, so, fd + 10240);
        dma1(r1, v1, so + 40 * F * 2, fd + 5120); dma1(r3, v1, so + 40 * F * 2, fd + 15360);
        BAR0();
        VMW(0);
        lds_inc(&ctl[13], lane);
      } else if (w < 2) {
        WAITC(12, n12);
        PACE(lds_ld(&ctl[26]) + 100 + TD + PW)
        put_unit(rl, (at_g(2 * ln + w) ^ pol) + (rg * 64 + lane) * 16, 0.f, 0.f, ~ta, ~tb);
      }
      const int tw = w < 2 ? w : w < 9 ? w - 2 : 7;
      unsigned bo = lb + L_FF + (tw >> 2) * 10240 + FRAG(tw & 3), b2 = bo + (g4 < 2 ? 8192 : 0);
      int go4 = lane < 16 ? (at_g(2 * ln + ((tw >> 1) & 1)) ^ pol) + (rg * 64 + (tw >> 2) * 32 + (tw & 1) * 16 + lane) * 16 : OOB;
      unsigned ao = lb + L_HS + 192 * (lane & 1) + 16 * g4;
      PINV(go4); PINV(ao); PINV(bo); PINV(b2);
      u32x4 af[3], bf[3];
      WAITC(13, 5u);
      bf[0] = frag(bo, 0, 512); bf[1] = frag(bo, 4096, 4096 + 512); bf[2] = frag(b2, 0, 512);
      PINV(bf[0]); PINV(bf[1]); PINV(bf[2]);
      VMW(0);
      BAR0();
#pragma unroll
      for (int ks = 0; ks < 3; ++ks) af[ks] = lds_ld4(ao, 64 * ks);
      PIN();
      f32x4 acc = {0.f, 0.f, 0.f, 0.f};
#pragma unroll
      for (int ks = 0; ks < 3; ++ks) acc = mma(af[ks], bf[ks], acc);
      put_unit(rl, go4, acc.x, acc.y, ta, tb);
    } else if (w == 13) {
      unsigned char* go = ws + (at_m3(2 * ln + (lane & 1), rg) ^ pol);
      unsigned mt = ta;
      PINV(go); PINV(mt);
      BAR0();
      RING(go, mt, 3ull);
      __builtin_amdgcn_raw_buffer_store_b64(u32x2{ta, tb ^ 0x9E3779B9u}, rl, (b == 0 && lane == 0) ? 0 : OOB, 0, SC1);
    } else if (w == 14) {
      unsigned hi_ = lb + L_HS + 4 * (lane < 40 ? lane : 0);
      int sto = lane < 8 ? (at_st(ln) ^ pol) + rg * 128 + lane * 16 : OOB;
      PINV(hi_); PINV(sto);
      PACE(lds_ld(&ctl[26]) + 100 + TD + PW)
      put_unit(rl, sto, 0.f, 0.f, ~ta, ~tb);
      VMW(0);
      BAR0();
      const unsigned hp = *(lds_u32)(size_t)hi_;
      const float h0 = lane < 40 ? lo16(hp) : 0.f, h1 = lane < 40 ? hi16(hp) : 0.f;
      const float cs = __uint_as_float(__builtin_amdgcn_readfirstlane(hp) << 16);
      const float d0 = lane < 40 ? h0 - cs : 0.f, d1 = lane < 40 ? h1 - cs : 0.f;
      float sr = red32(sw32(d0 + d1, d0 * d0 + d1 * d1));
      const float s2 = __uint_as_float(__builtin_amdgcn_readlane(__float_as_uint(sr), 32));
      const float mw = fmaf(sr, 1.f / 80.f, cs), m2 = fmaxf(fmaf(-sr * (1.f / 80.f), sr, s2), 0.f);
      put_unit(rl, sto, mw, m2, ta, tb);
    } else if (j < 5) {
      const rsrc_t rout = mk(out, (D * 2));
      const int cl = 16 * j + c8 + 8 * (lane >> 5);
      unsigned char* gi = ws + (at_m4(5 * (xcd >> 2) + j, lane < N5 ? lane : 0) ^ pol);
      int op = (lane & 24) ? OOB : (80 * xcd + cl) * 2;
      int po = WS_P5 + (at_row(r8, 5 * xcd + j) ^ pol) + c8 * 16;
      unsigned sl[4];
      PINV(gi); PINV(op); PINV(po);
      BAR0();
      float hv = lo16(reinterpret_cast<const u16*>(hs)[cl]);
      PINV(hv);
      __builtin_amdgcn_s_sleep(20);
      until(gate(gi, ta, (1ull << N5) - 1ull, sl) + D4);
      u32x4 v[6];
      load_units<6, 65536>(rl, po, v);
      PIN();
#define FIN_() { float s0 = F_(v[0].x), s1 = F_(v[0].z); bad = 0u; \
        _Pragma("unroll") for (int k = 0; k < 6; ++k) { if (k) { s0 += F_(v[k].x); s1 += F_(v[k].z); } \
          if (k < 5) { bad = __builtin_amdgcn_bitop3_b32(v[k].y, ta, bad, 0xBE); bad = __builtin_amdgcn_bitop3_b32(v[k].w, tb, bad, 0xBE); PINV(s0); PINV(s1); PINV(bad); PIN(); } } \
        s0 = sw32(s0, s1); \
        bad = __builtin_amdgcn_bitop3_b32(v[5].y, ta, bad, 0xBE); bad = __builtin_amdgcn_bitop3_b32(v[5].w, tb, bad, 0xBE); \
        s0 = sw16(s0, s0); s0 += dppf<ROR8>(s0); s0 = bfr(s0) + hv; res = pk2(s0, s0); }
      unsigned res, bad;
      FIN_()
      PINV(res);
      if (RARE(!none(bad != 0u))) {
        poll_units<6, 65536>(rl, po, v, ta, tb);
        FIN_()
      }
      __builtin_amdgcn_raw_buffer_store_b16((u16)res, rout, op, 0, 0);
      KEEP(sl);
      asm volatile("s_endpgm");
    }
  } else if (j < 28) {
    if (w == 3) {
      unsigned char* gi = ws + (at_m3(n5, c8) ^ pol);
      unsigned ai = lb + L_ACT + (lane < 32 ? 2 * lane : 64), fa = lb + L_CTL + 24 * 4;
      int go_ = (at_g(n5) ^ pol) + lane * 16, so_ = (at_st(n5 >> 1) ^ pol) + c8 * 128;
      unsigned sl[4], lvv = 1u;
      PINV(gi); PINV(ai); PINV(fa); PINV(go_); PINV(so_); PINV(lvv);
      WAITC(12, n12);
      wait_marks(gi, ta, 0xFFull, sl);
      u32x4 gv[8], sv[1];
      load_units<1, 0>(rl, so_, sv);
      load_units<8, 1024>(rl, go_, gv);
      PIN();
      lds_st(fa, lvv);
      PIN();
#define ACT_() { const float c0s = F_(__builtin_amdgcn_readfirstlane(sv[0].x));       \
        float da = F_(sv[0].x) - c0s, m2 = fmaf(80.f * da, da, F_(sv[0].z)); \
        da += dppf<XOR1>(da); m2 += dppf<XOR1>(m2); da += dppf<XOR2>(da); m2 += dppf<XOR2>(m2); da += dppf<HMIR>(da); m2 += dppf<HMIR>(m2); \
        da *= 0.125f; \
        const float var = bfr(fmaxf(fmaf(-da, da, m2 * (1.f / D)), 0.f)); \
        float mb = bfr(c0s + da), rd = __builtin_amdgcn_rcpf(bfr(__builtin_amdgcn_sqrtf(bfr(var + 1e-5f)))); \
        PINV(mb); PINV(rd); PIN(); \
        float gs_ = F_(gv[0].x), cs_ = F_(gv[0].z); \
        bad = __builtin_amdgcn_bitop3_b32(sv[0].y, ta, 0u, 0xBE); bad = __builtin_amdgcn_bitop3_b32(sv[0].w, tb, bad, 0xBE); \
        _Pragma("unroll") for (int k = 0; k < 8; ++k) { if (k) { gs_ += F_(gv[k].x); cs_ += F_(gv[k].z); } \
          if (k < 7) { bad = __builtin_amdgcn_bitop3_b32(gv[k].y, ta, bad, 0xBE); bad = __builtin_amdgcn_bitop3_b32(gv[k].w, tb, bad, 0xBE); PINV(gs_); PINV(cs_); PINV(bad); PIN(); } } \
        float val = bfr((gs_ - mb * cs_) * rd); \
        float sg = bfr(val * __builtin_amdgcn_rcpf(1.f + __builtin_amdgcn_exp2f(-val * L2E))); \
        const float av = sg * F_(__builtin_amdgcn_permlane32_swap(__float_as_uint(val), __float_as_uint(val), false, false)[1]);     \
        *(__attribute__((address_space(3))) u16*)(size_t)ai = (u16)pk2(av, av); \
        bad = __builtin_amdgcn_bitop3_b32(gv[7].y, ta, bad, 0xBE); bad = __builtin_amdgcn_bitop3_b32(gv[7].w, tb, bad, 0xBE); }
      unsigned bad;
      ACT_()
      if (RARE(!none(bad != 0u))) {
        poll_units<8, 1024>(rl, go_, gv, ta, tb); poll_units<1, 0>(rl, so_, sv, ta, tb);
        ACT_()
      }
      BAR();
      KEEP(sl);
    } else if (w == 2) {
      unsigned char* go = ws + (at_m4(lane < NG4 ? lane : 0, n5) ^ pol);
      PINV(go);
      WAITC(13, 7u);
      SPINC(24, 1u);
      announce(go, ta, (unsigned)__builtin_amdgcn_s_memrealtime(), ((1ull << NG4) - 1ull));
      BAR0();
    } else if (w >= 4 && w < 14) {
      if (w < 8) {
        const rsrc_t r2 = mk(w2, F * D * 2);
        const int q = w - 4, so = (R5 * n5 + 8 * q) * D * 2;
        const unsigned fd = lb + L_FF + q * 1024;
        if (W2E) {
          WAITC(22, 6u);
          WAITS(26, 1u);
          until((lds_ld(&ctl[26]) + TB + W2T));
#pragma unroll
          for (int i = 0; i < W2E; ++i) dma1(r2, vo, so + i * 128, fd + i * 4096);
        }
        BAR0();
        WAITC(12, n12);
#pragma unroll
        for (int i = W2E; i < 10; ++i) dma1(r2, vo, so + i * 128, fd + i * 4096);
        VMW(0);
        lds_inc(&ctl[13], lane);
      }
      const int fw = w - 4;
      int so = (lane & 8) ? OOB : WS_P5 + (at_row(n5, 4 * fw) ^ pol) + (8 * g4 + c8) * 16;
      WAITC(13, 7u);
      u32x4 bf[4];
#pragma unroll
      for (int t = 0; t < 4; ++t) bf[t] = frag(lb + L_FF + fw * 4096 + FRAG(t), 0, 512);
#pragma unroll
      for (int t = 0; t < 4; ++t) asm volatile("" : "+v"(bf[t]));
      unsigned ao = lb + L_ACT + 16 * g4;
      PINV(ao); PINV(so);
      BAR0();
      const u32x4 av = lds_ld4(ao, 0);
      const f32x4 z = {0.f, 0.f, 0.f, 0.f};
      f32x4 d0 = mma(av, bf[0], z), d1 = mma(av, bf[1], z), d2 = mma(av, bf[2], z), d3 = mma(av, bf[3], z);
      asm volatile("" : "+v"(d0), "+v"(d1), "+v"(d2), "+v"(d3));
      float c0 = __uint_as_float(__builtin_amdgcn_update_dpp(__float_as_uint(d0.x), __float_as_uint(d1.x), 0xE4, 0x2, 0xf, false));
      c0 = __uint_as_float(__builtin_amdgcn_update_dpp(__float_as_uint(c0), __float_as_uint(d2.x), 0xE4, 0x4, 0xf, false));
      c0 = __uint_as_float(__builtin_amdgcn_update_dpp(__float_as_uint(c0), __float_as_uint(d3.x), 0xE4, 0x8, 0xf, false));
      const float c1 = dppf<ROR8>(c0);
      put_unit(rl, so, c0, c1, ta, tb);
      PIN();
    } else {
      if (w != 15) {
        WAITC(12, n12);
        PACE(lds_ld(&ctl[26]) + 100 + TD + PW)
        const int pz = WS_P5 + at_row(n5, 0) + (w < 2 ? 8192 * w + lane * 16 : 2048 + (lane >> 5) * 8192 + (lane & 31) * 16);
        put_unit(rl, pz, 0.f, 0.f, ~ta, ~tb);
        put_unit(rl, w < 2 ? pz + 1024 : OOB, 0.f, 0.f, ~ta, ~tb);
        VMW(0);
        lds_inc(&ctl[13], lane);
      }
    }
  }
}
}

extern "C" void kernel_launch(void* const* d_in, const int* in_sizes, int n_in, void* d_out, int out_size, void* d_ws, size_t ws_size,
                              hipStream_t stream) {
  hipLaunchKernelGGL(k_fused, dim3(NWG), dim3(NTHR), 0, stream, (const u16*)d_in[0], (const u16*)d_in[1], (const u16*)d_in[3], (unsigned char*)d_ws,
                     (const u16*)d_in[2], (const u16*)d_in[4], (const u16*)d_in[6], (const u16*)d_in[7], (const u16*)d_in[8], (u16*)d_out);
}
```

```cpp
#include <hip/hip_runtime.h>
#include <cstdint>

namespace {
constexpr int D = 640, H = 32, DH = 192, SK = 32768, WIN = 4096, F = 1536;
constexpr int NWG = 256, NTHR = 1024;
constexpr int N5 = 48, R5 = F / N5;
constexpr int NST = 16, NREC = 8;
constexpr int WS_PQ = 256;
constexpr int WS_TO = WS_PQ + 320 * 32 * 16;
constexpr int WS_LB = WS_TO + 1024;
constexpr int WS_P1 = 11 * 16384;
constexpr int WS_P2 = WS_P1 + 16 * 16384;
constexpr int WS_G = WS_P2 + 16 * 16384;
constexpr int WS_ST = WS_G + 24 * 16384;
constexpr int WS_P5 = WS_ST + 2 * 16384;
constexpr int NG4 = 10;
constexpr int WS_GT = WS_P5 + 24 * 16384;
constexpr int WS_END = WS_GT + 2 * 16384;
static_assert(WS_LB + 8192 <= WS_P1, "ws");
constexpr int at_rec(int hd) { return WS_P1 + ((hd & 3) + 4 * (hd >> 3)) * 16384 + (hd & 4) * 2048; }
constexpr int at_row(int n, int tile) { return (n >> 1) * 16384 + (n & 1) * 4096 + (n & 6) * 256 + (tile < 20 ? tile * 128 : 8192 + (tile - 20) * 128); }
constexpr int at_g(int n) { return WS_G + (n < 24 ? n * 16384 : 8192 + (n - 24) * 16384); }
constexpr int at_st(int l) { return WS_ST + (l < 12 ? 0 : 8192) + ((l < 12 ? l : l - 12) >> 3) * 16384 + ((l < 12 ? l : l - 12) & 7) * 1024; }
constexpr int at_m3(int n, int i) { return WS_GT + (n < 24 ? n * 256 : 8192 + (n - 24) * 256) + i * 4; }
constexpr int at_m4(int c, int n) { return WS_GT + 16384 + (c < 5 ? c * 768 : 8192 + (c - 5) * 768) + (n >> 4) * 256 + (n & 15) * 4; }
constexpr int L_RING = 0;
constexpr int L_WO = 49152;
constexpr int L_FF = 2 * 49152;
constexpr int L_XS = L_FF, L_PART = L_FF + 2560;
constexpr int L_XRAW = 3 * 49152;
constexpr int L_P = L_XRAW + 1280;
constexpr int L_REC = L_P + 1024;
constexpr int L_QS = L_REC + 8 * 416;
constexpr int L_HS = L_QS + 384;
constexpr int L_ACT = L_HS + 384;
constexpr int L_WMAX = L_ACT + 80, L_LSUM = L_WMAX + 64;
constexpr int L_WRE = L_LSUM + 64;
constexpr int L_CTL = L_WRE + 64;
constexpr int L_END = L_CTL + 128;
static_assert(L_END <= 163840, "lds");
constexpr unsigned NK = NST - 1;
constexpr unsigned SPIN_CAP = 1u << 18;
constexpr int P0 = 96, RK = 164, RV = 167, RV2 = 147, GK = 10, SLK = 15, SLV = 18, THK = 14, THV = 12;
constexpr int LBT = 85, LB0 = 10, SRD = 155, SRV = 150, TW = 70, WQD = 16, TL = 36, TB = 165, TY = 105;
constexpr int F1D = 25, F1S = 170, NE = 2, W2T = 22, TD = 178, PW = 56, D4 = 54, PT = 110, LBK = 67;
constexpr int FS = 10, W2B = 6, QM = 9;

typedef unsigned short u16;
typedef unsigned u32x2 __attribute__((ext_vector_type(2)));
typedef unsigned u32x4 __attribute__((ext_vector_type(4)));
typedef float f32x2 __attribute__((ext_vector_type(2)));
typedef float f32x4 __attribute__((ext_vector_type(4)));
typedef __attribute__((ext_vector_type(2))) __bf16 bf16x2;
typedef __amdgpu_buffer_rsrc_t rsrc_t;

__device__ __forceinline__ float lo16(unsigned u) { return __uint_as_float(u << 16); }
__device__ __forceinline__ float hi16(unsigned u) { return __uint_as_float(u & 0xFFFF0000u); }
__device__ __forceinline__ unsigned pk2(float a, float b) {
  f32x2 v = {a, b};
  return __builtin_bit_cast(unsigned, __builtin_convertvector(v, bf16x2));
}
__device__ __forceinline__ float bfr(float f) { return __uint_as_float(pk2(0.f, f)); }
__device__ __forceinline__ float dot2(unsigned a, unsigned b, float acc) {
  return __builtin_amdgcn_fdot2_f32_bf16(__builtin_bit_cast(bf16x2, a), __builtin_bit_cast(bf16x2, b), acc, false);
}
template <int CTRL> __device__ __forceinline__ float dppf(float x) {
  return __builtin_bit_cast(float, __builtin_amdgcn_mov_dpp(__builtin_bit_cast(int, x), CTRL, 0xf, 0xf, true));
}
constexpr int XOR1 = 0xB1, XOR2 = 0x4E, HMIR = 0x141, MIR = 0x140, ROR8 = 0x128;
__device__ __forceinline__ float sw16(float a, float b) {
  auto s = __builtin_amdgcn_permlane16_swap(__float_as_uint(a), __float_as_uint(b), false, false);
  return __uint_as_float(s[0]) + __uint_as_float(s[1]);
}
__device__ __forceinline__ float sw32(float a, float b) {
  auto s = __builtin_amdgcn_permlane32_swap(__float_as_uint(a), __float_as_uint(b), false, false);
  return __uint_as_float(s[0]) + __uint_as_float(s[1]);
}
__device__ __forceinline__ float red16(float x) {
  x += dppf<XOR1>(x); x += dppf<XOR2>(x); x += dppf<HMIR>(x); x += dppf<MIR>(x); return x;
}
__device__ __forceinline__ float red32(float x) { x = red16(x); return sw16(x, x); }
__device__ __forceinline__ float red64(float x) { x = red32(x); return sw32(x, x); }
__device__ __forceinline__ float max16(float x) {
  x = fmaxf(x, dppf<XOR1>(x)); x = fmaxf(x, dppf<XOR2>(x)); x = fmaxf(x, dppf<HMIR>(x)); x = fmaxf(x, dppf<MIR>(x)); return x;
}
__device__ __forceinline__ float max64(float x) {
  x = max16(x);
  auto s = __builtin_amdgcn_permlane16_swap(__float_as_uint(x), __float_as_uint(x), false, false);
  x = fmaxf(__uint_as_float(s[0]), __uint_as_float(s[1]));
  auto t = __builtin_amdgcn_permlane32_swap(__float_as_uint(x), __float_as_uint(x), false, false);
  return fmaxf(__uint_as_float(t[0]), __uint_as_float(t[1]));
}
template <int CTRL> __device__ __forceinline__ float rs(float a, float b, bool sel) {
  const float keep = sel ? b : a, send = sel ? a : b;
  return keep + dppf<CTRL>(send);
}
__device__ __forceinline__ unsigned plo(unsigned r0, unsigned r1) { return __builtin_amdgcn_perm(r1, r0, 0x05040100u); }
__device__ __forceinline__ unsigned phi(unsigned r0, unsigned r1) { return __builtin_amdgcn_perm(r1, r0, 0x07060302u); }
__device__ __forceinline__ rsrc_t mk(const void* p, int bytes) {
  return __builtin_amdgcn_make_buffer_rsrc(const_cast<void*>(p), 0, bytes, 0x00020000);
}
#define BAR() asm volatile("s_waitcnt lgkmcnt(0)\n\ts_barrier" ::: "memory")
#define PIN() __builtin_amdgcn_sched_barrier(0)
constexpr int SC1 = 16, NT = 2, OOB = 0x40000000;

template <int AUX = SC1>
__device__ __forceinline__ void put_unit(rsrc_t rw, int off, float v0, float v1, unsigned ta, unsigned tb) {
  u32x4 u = {__float_as_uint(v0), ta, __float_as_uint(v1), tb};
  __builtin_amdgcn_raw_buffer_store_b128(u, rw, off, 0, AUX);
}
template <int N, int STRIDE, int AUX = SC1>
__device__ __forceinline__ void load_units(rsrc_t rw, int voff, u32x4 (&v)[N]) {
#pragma unroll
  for (int k = 0; k < N; ++k) v[k] = __builtin_amdgcn_raw_buffer_load_b128(rw, voff, k * STRIDE, AUX);
}
template <int N>
__device__ __forceinline__ unsigned units_bad(const u32x4 (&v)[N], unsigned ta, unsigned tb, unsigned acc = 0u) {
  unsigned acc2 = 0u;
#pragma unroll
  for (int k = 0; k < N; ++k) { acc = __builtin_amdgcn_bitop3_b32(v[k].y, ta, acc, 0xBE); acc2 = __builtin_amdgcn_bitop3_b32(v[k].w, tb, acc2, 0xBE); }
  return acc | acc2;
}
__device__ __forceinline__ bool none(bool p) { return __builtin_amdgcn_ballot_w64(p) == 0ull; }
#define RARE(c) __builtin_expect(!!(c), 0)
template <int N, int STRIDE, int AUX = SC1>
__device__ __forceinline__ unsigned poll_units(rsrc_t rw, int voff, u32x4 (&v)[N], unsigned ta, unsigned tb, bool live = true) {
  unsigned spins = 0;
#pragma unroll 1
  for (; spins < SPIN_CAP; ++spins) {
    asm volatile("" : "+v"(voff) :: "memory");
    load_units<N, STRIDE, AUX>(rw, voff, v);
    if (none(units_bad<N>(v, ta, tb) != 0u && live)) break;
  }
  return spins;
}
__device__ __forceinline__ unsigned lds_ld(unsigned* p) {
  return __builtin_amdgcn_readfirstlane(__hip_atomic_load(p, __ATOMIC_RELAXED, __HIP_MEMORY_SCOPE_WORKGROUP));
}
typedef __attribute__((address_space(3))) unsigned* lds_u32;
__device__ __forceinline__ void lds_inc(unsigned* p, int lane, unsigned n = 1u) {
  asm volatile("s_waitcnt lgkmcnt(0)\n\ts_mov_b64 exec, 1\n\tds_add_u32 %0, %1\n\ts_mov_b64 exec, -1" :: "v"((unsigned)(size_t)(lds_u32)p), "v"(n) : "memory");
}
__device__ __forceinline__ void pv_pair(float (&a)[24], const u32x4* A, const u32x4* B, const unsigned (&xp)[3]) {
#pragma unroll
  for (int jj = 0; jj < 3; ++jj)
#pragma unroll
    for (int i = 0; i < 4; ++i) {
      a[8 * jj + 2 * i] = dot2(plo(A[jj][i], B[jj][i]), xp[jj], a[8 * jj + 2 * i]);
      a[8 * jj + 2 * i + 1] = dot2(phi(A[jj][i], B[jj][i]), xp[jj], a[8 * jj + 2 * i + 1]);
    }
}
__device__ __forceinline__ f32x4 red_rows(const float (&a)[24], bool t1) {
  float bq[12], v[6];
#pragma unroll
  for (int k = 0; k < 12; ++k) bq[k] = sw32(a[k], a[k + 12]);
#pragma unroll
  for (int k = 0; k < 6; ++k) { v[k] = sw16(bq[k], bq[k + 6]); v[k] += dppf<ROR8>(v[k]); }
  return f32x4{t1 ? v[4] : v[0], t1 ? v[5] : v[1], v[2], v[3]};
}
template <int N, int STRIDE>
__device__ __forceinline__ unsigned poll_lane(rsrc_t rw, int voff, u32x4 (&v)[N], unsigned ta, unsigned tb) {
  unsigned spins = 0;
#pragma unroll 1
  for (; spins < SPIN_CAP; ++spins) {
    if (none(units_bad<N>(v, ta, tb) != 0u)) break;
#pragma unroll
    for (int k = 0; k < N; ++k) {
      const unsigned long long em = __builtin_amdgcn_ballot_w64(((v[k].y ^ ta) | (v[k].w ^ tb)) != 0u);
      asm volatile("s_mov_b64 exec, %3\n\tbuffer_load_dwordx4 %0, %1, %2, %4 offen sc1\n\ts_mov_b64 exec, -1" : "+v"(v[k]) : "v"(voff), "s"(rw), "s"(em), "s"(k * STRIDE) : "memory");
    }
    if constexpr (N == 2) asm volatile("s_waitcnt vmcnt(0)" : "+v"(v[0]), "+v"(v[1]) :: "memory");
    else asm volatile("s_waitcnt vmcnt(0)" : "+v"(v[0]), "+v"(v[1]), "+v"(v[2]), "+v"(v[3]) :: "memory");
  }
  return spins;
}
#define SL_ "s_sleep 2\n\t"
#define RING(p, t, em) asm volatile("s_mov_b64 exec, %2\n\tglobal_atomic_swap %0, %1, off sc1\n\ts_mov_b64 exec, -1" :: "v"(p), "v"(t), "s"(em) : "memory")
__device__ __forceinline__ void wait_marks(const void* p, unsigned tg, unsigned long long em, unsigned (&s)[4]) {
  unsigned cnt = SPIN_CAP / 4;
#define A1_(a) "global_atomic_add %[" #a "], %[p], %[z], off sc0 sc1\n\t"
#define CH_(a) "s_waitcnt vmcnt(3)\n\tv_cmpx_ne_u32 exec, %[" #a "], %[tg]\n\ts_cbranch_execz 2f\n\t" SL_
  asm volatile(
      "s_mov_b64 exec, %[em]\n\t"
      A1_(a) SL_ A1_(b) SL_ A1_(c) SL_
      "1:\n\t"
      A1_(d) CH_(a)
      A1_(a) CH_(b)
      A1_(b) CH_(c)
      A1_(c) CH_(d)
      "s_sub_u32 %[cnt], %[cnt], 1\n\t"
      "s_cmp_lg_u32 %[cnt], 0\n\t"
      "s_cbranch_scc1 1b\n\t"
      "2:\n\t"
      "s_mov_b64 exec, -1"
      : [cnt] "+s"(cnt), [a] "=&v"(s[0]), [b] "=&v"(s[1]), [c] "=&v"(s[2]), [d] "=&v"(s[3])
      : [p] "v"(p), [z] "v"(0u), [tg] "v"(tg), [em] "s"(em)
      : "memory", "vcc", "scc");
}
__device__ __forceinline__ void announce(const void* p, unsigned tg, unsigned t, unsigned long long em) {
  asm volatile("s_mov_b64 exec, %2\n\tglobal_atomic_swap %0, %1, off sc1\n\ts_mov_b64 exec, -1" :: "v"(p), "v"((tg << 12) | (t & 0xFFFu)), "s"(em) : "memory");
}
__device__ __forceinline__ unsigned gate(const void* p, unsigned tg, unsigned long long em, unsigned (&s)[4]) {
  unsigned cnt = SPIN_CAP / 4, res = 0u, t;
#define CI_(a) "s_waitcnt vmcnt(3)\n\tv_mov_b32 %[res], %[" #a "]\n\tv_lshrrev_b32 %[t], 12, %[" #a "]\n\tv_cmpx_ne_u32 exec, %[t], %[tg]\n\ts_cbranch_execz 2f\n\ts_sleep 1\n\t"
  asm volatile(
      "s_mov_b64 exec, %[em]\n\t"
      A1_(a) SL_ A1_(b) SL_ A1_(c) SL_
      "1:\n\t"
      A1_(d) CI_(a)
      A1_(a) CI_(b)
      A1_(b) CI_(c)
      A1_(c) CI_(d)
      "s_sub_u32 %[cnt], %[cnt], 1\n\t"
      "s_cmp_lg_u32 %[cnt], 0\n\t"
      "s_cbranch_scc1 1b\n\t"
      "2:\n\t"
      "s_mov_b64 exec, -1"
      : [cnt] "+s"(cnt), [res] "+&v"(res), [t] "=&v"(t), [a] "=&v"(s[0]), [b] "=&v"(s[1]), [c] "=&v"(s[2]), [d] "=&v"(s[3])
      : [p] "v"(p), [z] "v"(0u), [tg] "v"(tg & 0xFFFFFu), [em] "s"(em)
      : "memory", "vcc", "scc");
  const unsigned now = (unsigned)__builtin_amdgcn_s_memrealtime();
  const float age = max64(-(float)(res ? (now - res) & 0xFFFu : 0xFFFu));
  return now + (unsigned)__builtin_amdgcn_readfirstlane((int)age);
}
__device__ __forceinline__ void until(unsigned due) {
  while (due - (unsigned)__builtin_amdgcn_s_memrealtime() - 1u < 400u) {}
}
#define KEEP(s) asm volatile("" :: "v"(s[0]), "v"(s[1]), "v"(s[2]), "v"(s[3]) : "memory")
typedef short s16x4 __attribute__((ext_vector_type(4)));
typedef __attribute__((ext_vector_type(8))) __bf16 bf16x8;
typedef __attribute__((address_space(3))) s16x4* lds_s16x4;
__device__ __forceinline__ u32x4 frag(unsigned a, int off, int off_hi) {
  const s16x4 lo = __builtin_amdgcn_ds_read_tr16_b64_v4i16((lds_s16x4)(size_t)(a + off));
  const s16x4 hi = __builtin_amdgcn_ds_read_tr16_b64_v4i16((lds_s16x4)(size_t)(a + off_hi));
  const u32x2 l2 = __builtin_bit_cast(u32x2, lo), h2 = __builtin_bit_cast(u32x2, hi);
  return u32x4{l2.x, l2.y, h2.x, h2.y};
}
__device__ __forceinline__ f32x4 mma(u32x4 a, u32x4 b, f32x4 c) {
  return __builtin_amdgcn_mfma_f32_16x16x32_bf16(__builtin_bit_cast(bf16x8, a), __builtin_bit_cast(bf16x8, b), c, 0, 0, 0);
}
typedef const __attribute__((address_space(3))) u32x4* lds_cv4;
__device__ __forceinline__ unsigned lds_addr(const void* p) { return (unsigned)(size_t)(const __attribute__((address_space(3))) void*)p; }
__device__ __forceinline__ u32x4 lds_ld4(unsigned a, int off) { return *(lds_cv4)(size_t)(a + off); }
__device__ __forceinline__ void lds_st(unsigned a, unsigned v) { *(lds_u32)(size_t)a = v; }
#define PINV(x) asm volatile("" : "+v"(x))
#define WAITC(i, n) while (lds_ld(&ctl[i]) < (n)) __builtin_amdgcn_s_sleep(1); asm volatile("" ::: "memory")
#define WAITS(i, n) while (lds_ld(&ctl[i]) < (n)) __builtin_amdgcn_s_sleep(4); asm volatile("" ::: "memory")
#define SPINC(i, n) while (lds_ld(&ctl[i]) < (n)) {} asm volatile("" ::: "memory")

__device__ __forceinline__ void dma1(rsrc_t r, int voff, int soff, unsigned lds) {
  asm volatile("s_mov_b32 m0, %0\n\ts_nop 0\n\tbuffer_load_dwordx4 %1, %2, %3 offen nt lds" :: "s"(lds), "v"(voff), "s"(r), "s"(soff) : "memory");
}
__device__ __forceinline__ void dma3(rsrc_t r, int voff, int soff, unsigned lds) {
  asm volatile("s_mov_b32 m0, %0\n\ts_nop 0\n\tbuffer_load_dwordx4 %1, %2, %3 offen nt lds\n\t"
               "s_add_u32 m0, m0, 896\n\ts_nop 0\n\tbuffer_load_dwordx4 %1, %2, %3 offen offset:128 nt lds\n\t"
               "s_add_u32 m0, m0, 896\n\ts_nop 0\n\tbuffer_load_dwordx4 %1, %2, %3 offen offset:256 nt lds" :: "s"(lds), "v"(voff), "s"(r), "s"(soff) : "memory", "scc");
}
#define VMW(n) asm volatile("s_waitcnt vmcnt(" #n ")" ::: "memory")
#define BAR0() asm volatile("s_barrier" ::: "memory")

__device__ __forceinline__ void code_ahead(int i) {
  asm volatile("s_getpc_b64 s[88:89]\n1:\n\t"
               "s_add_u32 s88, s88, .Lkf_begin - 1b\n\ts_addc_u32 s89, s89, -1\n\ts_and_b32 s89, s89, 0xffff\n\t"
               "s_mov_b32 s90, 28672\n\ts_mov_b32 s91, 0x20000\n\t"
               "s_buffer_load_dword s92, s[88:91], %0 offset:0\n\ts_buffer_load_dword s92, s[88:91], %0 offset:64\n\t"
               "s_buffer_load_dword s92, s[88:91], %0 offset:128\n\ts_buffer_load_dword s92, s[88:91], %0 offset:192\n\t"
               "s_buffer_load_dword s92, s[88:91], %0 offset:256\n\ts_buffer_load_dword s92, s[88:91], %0 offset:320\n\t"
               "s_buffer_load_dword s92, s[88:91], %0 offset:384\n\ts_waitcnt lgkmcnt(0)"
               :: "s"(i * 448) : "memory", "scc", "s88", "s89", "s90", "s91", "s92");
}
__device__ __forceinline__ void survey(rsrc_t rw, int off, int slot, unsigned* cnt, unsigned c0, unsigned dc, unsigned t0, unsigned dt, unsigned ta, unsigned tb, int lane) {
#define NOW() (unsigned)__builtin_amdgcn_s_memrealtime()
#define PACE(due) { PIN(); const unsigned d_ = (due); while ((int)(NOW() - d_) < 0) __builtin_amdgcn_s_sleep(1); PIN(); }
  if (slot >= 0) {
    unsigned a = 0u;
    int l0 = 0, l1 = 0;
#pragma unroll 1
    for (unsigned i = 0; i < 5; ++i) {
      if (i < 4) while (lds_ld(cnt) < c0 + i * dc) __builtin_amdgcn_s_sleep(1);
      asm volatile("buffer_load_dword %0, %1, %2, 0 offen\n\ts_waitcnt vmcnt(1)" : "+v"(a) : "v"(OOB), "s"(rw) : "memory");
      const int l = min((int)(NOW() - t0 - (i - 1) * dt / 4), 130);
      if (i >= 3) l1 += l; else if (i) l0 += l;
    }
    put_unit(rw, lane < 8 ? off + slot * 128 + lane * 16 : OOB, 0.5f * (float)l0, 0.5f * (float)l1, ta, tb);
    asm volatile("s_waitcnt vmcnt(1)" :: "v"(a) : "memory");
  }
}
__device__ __forceinline__ f32x4 result(rsrc_t rw, int off, unsigned t2, unsigned ta, unsigned tb, int lane) {
  PACE(t2)
  u32x4 v[1];
  load_units<1, 0>(rw, off + (lane & 31) * 128, v);
  const unsigned u0 = v[0].x, u1 = v[0].z;
  const bool ok = units_bad<1>(v, ta, tb) == 0u, in = ok && ((lane & 31) >= 21) == (lane >= 32);
  const float n = red32(ok ? 1.f : 0.f), rn = __builtin_amdgcn_rcpf(fmaxf(n, 1.f));
  const float cl = fmaf(red32(ok ? __uint_as_float(u0) + __uint_as_float(u1) : 0.f), 0.5f * rn, 14.f);
  const float x0 = fminf(__uint_as_float(u0), cl), x1 = fminf(__uint_as_float(u1), cl);
  const float m = n < 16.f ? 1000.f : red32(ok ? (lane < 32 ? x0 : x1) : 0.f) * rn;
  const float g = red32(in ? x0 + x1 : 0.f) * __builtin_amdgcn_rcpf(fmaxf(red32(in ? 2.f : 0.f), 1.f));
#define RL_(x, l) __uint_as_float(__builtin_amdgcn_readlane(__float_as_uint(x), l))
  return f32x4{RL_(m, 0), RL_(m, 32), RL_(g, 32) - RL_(g, 0), 0.f};
}
__global__ __launch_bounds__(NTHR, 4) void k_fused(const u16* x_, const u16* kc_, const u16* wq_, unsigned char* ws_, const u16* vc_, const u16* wo_,
                                                   const u16* w1_, const u16* w3_, const u16* w2_, u16* out_) {
  typedef unsigned u32x8 __attribute__((ext_vector_type(8)));
  typedef __attribute__((address_space(1))) unsigned char* gp_t;
  const unsigned long long kap = (unsigned long long)__builtin_amdgcn_kernarg_segment_ptr();
  u32x8 ka, kb;
  u32x4 kc4;
  asm volatile(".Lkf_begin:\n\ts_load_dword s95, %0, 0x0" :: "s"(kap) : "memory", "s95");
#define ARGS() asm volatile("s_load_dwordx8 %0, %1, 0x0\n\ts_waitcnt lgkmcnt(0)" : "=&s"(ka) : "s"(kap) : "memory", "s95")
#define ARGS2() asm volatile("s_load_dwordx8 %0, %2, 0x20\n\ts_load_dwordx4 %1, %2, 0x40\n\ts_waitcnt lgkmcnt(0)" : "=&s"(kb), "=&s"(kc4) : "s"(kap) : "memory")
#define ARG(v, i) ((gp_t)(((unsigned long long)v[2 * (i) + 1] << 32) | v[2 * (i)]))
  __shared__ __attribute__((aligned(1024))) unsigned char smem[L_END];
  float* const xs = reinterpret_cast<float*>(smem + L_XS);
  unsigned* const xraw = reinterpret_cast<unsigned*>(smem + L_XRAW);
  f32x4* const part = reinterpret_cast<f32x4*>(smem + L_PART);
  unsigned* const qs = reinterpret_cast<unsigned*>(smem + L_QS);
  unsigned* const hs = reinterpret_cast<unsigned*>(smem + L_HS);
  float* const wmax = reinterpret_cast<float*>(smem + L_WMAX);
  float* const lsum = reinterpret_cast<float*>(smem + L_LSUM);
  unsigned* const ctl = reinterpret_cast<unsigned*>(smem + L_CTL);
  const unsigned lb = lds_addr(smem);
  __builtin_amdgcn_s_setprio(3);
  const float L2E = 1.4426950408889634f;
  const int tid = threadIdx.x, lane = tid & 63, r8 = lane >> 3, c8 = lane & 7;
  int w = __builtin_amdgcn_readfirstlane(tid >> 6);
  const int b = blockIdx.x, xcd = b & 7, j = b >> 3, h = xcd + 8 * (j >> 3), s = j & 7;
  const bool lt = xcd & 1;
  const bool one = lt || j < 16;
#define BLK(i) ((h * SK + (SK - WIN) + ((i) * 8 + s) * 32) * (DH * 2))
#define LDK() { const rsrc_t rk = mk((const void*)ARG(ka, 1), H * SK * DH * 2); \
    _Pragma("unroll") for (int g = 0; g < 4; ++g) _Pragma("unroll") for (int jj = 0; jj < 3; ++jj) \
    K[3 * g + jj] = __builtin_amdgcn_raw_buffer_load_b128(rk, vkv + jj * 128, sb + g * 3072, NT); }
#define SHIFT(hd, r, t) (((int)(((hd) * (r) / 32u + (r) - (((t) & 0xFFFFFFFu) * 4u) % (r)) % (r)) - (int)(r) / 2) / 4)
#define DUE(n) (tz + (n) * pd / 4)
#define ORG(t) while ((t = lds_ld(&ctl[30])) == 0u) __builtin_amdgcn_s_sleep(1)
#define KPACE(n) { ORG(tz); tz += SHIFT(h, (unsigned)pd, tz) + P0; PACE(DUE(n) - 8) PACE(DUE(n) + lds_ld(&ctl[6]) + (n) * lds_ld(&ctl[7]) / 4) }
  unsigned ta = 0u, tb = 0u;
  unsigned tz;
  const int pw = (w + 14) & 15, vkv = r8 * 384 + c8 * 16, sb = BLK(pw);
  const bool t1 = (lane >> 3) & 1;
#define PAIRS() const int k0 = 6 * (lane >> 4) + ((lane >> 1) & 4), k2 = (lane & 8) ? k0 : k0 + 2; \
  const int pr0 = 32 * (k0 >> 3) + 4 * c8 + ((k0 & 7) >> 1), pr1 = 32 * (k2 >> 3) + 4 * c8 + ((k2 & 7) >> 1)
  if (tid < 32) ctl[tid] = 0u;
  int sq[3], sg = w < 2 ? 8 * (H * DH * 2) : 3072, vq = r8 * (sg >> 3) + c8 * 16;
#pragma unroll
  for (int jj = 0; jj < 3; ++jj) {
    const int c = 6 * xcd + 3 * w + jj;
    int t = (c & 15) * 40 * (H * DH * 2) + j * (DH * 2) + (c >> 4) * 128;
    asm volatile("" : "+s"(t));
    sq[jj] = w < 2 ? t : sb + jj * 128;
    asm volatile("" : "+s"(sq[jj]));
  }
  asm volatile("" : "+s"(sg));
  int vq4 = w < 2 ? vq : OOB, vx = w < 2 ? lane * 4 : OOB;
  PINV(vq); PINV(vq4); PINV(vx);
  unsigned xr[5];
  u32x4 W[15];
#define K W
  if (w >= 2) __builtin_amdgcn_s_setprio(2);
  PIN();
  ARGS();
  asm volatile("s_cmp_lt_u32 %0, 2\n\ts_cbranch_scc1 1f\n\ts_barrier\n\t1:" : "+s"(w) :: "scc", "memory");
  if (w < 3) {
    const rsrc_t rx = mk((const void*)ARG(ka, 0), D * 2), rq = mk((const void*)(w < 2 ? ARG(ka, 2) : ARG(ka, 1)), w < 2 ? D * H * DH * 2 : H * SK * DH * 2);
#pragma unroll
    for (int k = 0; k < 5; ++k) xr[k] = __builtin_amdgcn_raw_buffer_load_b32(rx, vx + k * 256, 0, 0);
    PIN();
#pragma unroll
    for (int g = 0; g < 5; ++g) {
#pragma unroll
      for (int jj = 0; jj < 3; ++jj)
        W[3 * g + jj] = __builtin_amdgcn_raw_buffer_load_b128(rq, g < 4 ? vq : vq4, sq[jj] + g * sg, NT);
      PIN();
    }
  }
  asm volatile("s_cmp_ge_u32 %0, 2\n\ts_cbranch_scc1 1f\n\ts_barrier\n\t1:" : "+s"(w) :: "scc", "memory");
  if (w == 0) { ctl[30] = NOW() | 1u; }
  unsigned long long eps;
  asm volatile("s_cmp_lg_u32 %2, 12\n\ts_cbranch_scc1 1f\n\ts_load_dwordx2 %0, %1, 0x0\n\ts_waitcnt lgkmcnt(0)\n\t1:" : "=&s"(eps) : "s"(ARG(ka, 3)), "s"(w) : "memory", "scc");
  if (w >= 14) code_ahead(2 * j + (w & 1));
  if (w == 10 && (j & 1) && j < 30) {
    const int i = j >> 1, ai = i < 4 ? 0 : i < 9 ? 1 : (i - 5) >> 1;
    unsigned o = i < 4 ? ((xcd + 8 * i) * SK + SK - 1) * (DH * 2) : (i < 9 ? i - 4 : i & 1) * (D * F * 2 - 4096) + xcd * 512;
    asm volatile("s_load_dwordx2 s[88:89], %1, %2\n\ts_waitcnt lgkmcnt(0)\n\tglobal_load_dword %0, %0, s[88:89] nt\n\ts_waitcnt vmcnt(0)" : "+v"(o) : "s"(kap), "s"(32 + 8 * ai) : "memory", "s88", "s89");
  }
  if (w > 2 && pw < QM) {
    const int pd = RK;
    KPACE(pw)
    LDK()
    PIN();
    lds_inc(&ctl[4], lane);
#define SLOT() (s == ((h * 6) & 7) ? h : -1)
    if ((pw >> 1) == 1 && SLOT() >= 0) {
      const unsigned t0 = NOW();
      VMW(0);
      const unsigned l = min(NOW() - t0, 120u);
      if (pw == 2) ctl[14] = l;
      else {
        WAITC(5, 1u);
        put_unit(mk((unsigned char*)ARG(ka, 3), WS_END), lane < 8 ? WS_LB + h * 128 + lane * 16 : OOB, (float)lds_ld(&ctl[14]), (float)l, lds_ld(&ctl[0]), lds_ld(&ctl[1]));
      }
    }
  }
  asm volatile("" : "+s"(w));
  if (w < 2) {
    unsigned char* ws = (unsigned char*)ARG(ka, 3);
    const rsrc_t rw = mk(ws, WS_END);
    __builtin_amdgcn_s_setprio(0);
    PAIRS();
    if (w == 1 && lane < 56) hs[40 + lane] = (lane >= 8 && lane < 48) ? 0x3F803F80u : 0u;
    PIN();
    {
      float sm = 0.f;
#pragma unroll
      for (int k = 0; k < 5; ++k) sm += lo16(xr[k]) + hi16(xr[k]);
      const float mean = red64(sm) * (1.f / D);
      float q2 = 0.f;
#pragma unroll
      for (int k = 0; k < 5; ++k) { const float a = lo16(xr[k]) - mean, bb = hi16(xr[k]) - mean; q2 += a * a + bb * bb; }
      const float var = bfr(red64(q2) * (1.f / D));
      const float mb = bfr(mean), den = bfr(sqrtf(bfr(var + 1e-5f)));
#pragma unroll
      for (int k = 0; k < 5; ++k) {
        f32x2 v = {bfr(bfr(lo16(xr[k]) - mb) / den), bfr(bfr(hi16(xr[k]) - mb) / den)};
        *reinterpret_cast<f32x2*>(&xs[2 * (lane + 64 * k)]) = v;
        xraw[lane + 64 * k] = xr[k];
      }
    }
    __builtin_amdgcn_fence(__ATOMIC_RELEASE, "wavefront");
    __builtin_amdgcn_wave_barrier();
    {
      unsigned x01[3], x23[3], x4[3];
#pragma unroll
      for (int jj = 0; jj < 3; ++jj) {
        const float* xp = &xs[((6 * xcd + 3 * w + jj) & 15) * 40 + r8];
        x01[jj] = pk2(xp[0], xp[8]); x23[jj] = pk2(xp[16], xp[24]); x4[jj] = pk2(xp[32], 0.f);
      }
      float a[24];
#pragma unroll
      for (int k = 0; k < 24; ++k) a[k] = 0.f;
      pv_pair(a, &W[0], &W[3], x01);
      pv_pair(a, &W[6], &W[9], x23);
      pv_pair(a, &W[12], &W[12], x4);
      const f32x4 r4 = red_rows(a, t1);
      f32x2* const pp2 = reinterpret_cast<f32x2*>(part + 48 * w);
      if (!t1) pp2[pr1] = f32x2{r4.z, r4.w};
      pp2[pr0] = f32x2{r4.x, r4.y};
    }
    lds_inc(&ctl[2], lane);
    if (!w) {
      WAITC(5, 1u);
      ta = lds_ld(&ctl[0]); tb = lds_ld(&ctl[1]);
      while (lds_ld(&ctl[2]) != 2u) {}
      asm volatile("" ::: "memory");
      const int cl = (6 * xcd >> 4) + (lane >> 5);
      f32x2 a2 = {0.f, 0.f};
#pragma unroll
      for (int i = 0; i < 6; ++i) { const f32x2 t = reinterpret_cast<const f32x2*>(part)[32 * i + (lane & 31)]; if (((6 * xcd + i) >> 4) == cl) a2 += t; }
      int qo = cl <= ((6 * xcd + 5) >> 4) ? WS_PQ + j * 5120 + (xcd + (xcd > 2) + (xcd > 5)) * 512 + lane * 16 : OOB;
      PINV(qo); PINV(a2);
      put_unit<SC1 | NT>(rw, qo, a2.x, a2.y, ta, tb);
    }
    __builtin_amdgcn_s_setprio(2);
  }
  asm volatile("" : "+s"(w));
  if (w < 2 || w >= QM + 2) {
    const int pd = RK;
    if (w == 13 && j == ((5 * xcd + 2) & 7) * 4 + (xcd & 3)) {
      const unsigned long long pr = (unsigned long long)ARG(ka, 3) + WS_END;
      asm volatile("s_load_dword s88, %0, 0x0\n\ts_load_dword s89, %0, 0x2000\n\ts_load_dword s90, %0, 0x5000\n\ts_load_dword s91, %0, 0x7000\n\ts_waitcnt lgkmcnt(0)"
                   :: "s"(pr + ((xcd + 1) & 7) * 256) : "memory", "s88", "s89", "s90", "s91");
      unsigned t0;
      ORG(t0);
      PACE(t0 + PT)
      int d = 0;
#pragma unroll 1
      for (int i = 0; i < 4; ++i) {
        unsigned long long c0, c1;
        asm volatile("s_memtime %0\n\ts_waitcnt lgkmcnt(0)\n\ts_load_dword s88, %2, %3\n\ts_waitcnt lgkmcnt(0)\n\ts_memtime %1\n\ts_waitcnt lgkmcnt(0)"
                     : "=&s"(c0), "=&s"(c1) : "s"(pr + xcd * 256), "s"((i & 1) * 0x2000 + (i >> 1) * 0x5000) : "memory", "s88");
        const int t = min((int)(c1 - c0), 1200);
        d = -d + t;
      }
      WAITC(5, 1u);
      put_unit(mk((unsigned char*)ARG(ka, 3), WS_END), lane < 8 ? WS_TO + xcd * 128 + lane * 16 : OOB, __uint_as_float(t0), (float)(xcd < 4 ? -d : d), lds_ld(&ctl[0]), lds_ld(&ctl[1]));
    }
#define JUDGE(m, th, sl, lv) const int sl = ((m.y - m.x > th && m.y > 86.5f) || m.x > 500.f) ? 1 : 0, lv = min(max((int)m.x - LBT, 0) * LB0 / 8, 40)
    if (w == 12) {
      eps += 1ull;
      ta = (unsigned)eps; tb = (unsigned)(eps >> 32) ^ 0x9E3779B9u;
      if (lane == 0) { ctl[0] = ta; ctl[1] = tb; }
      lds_inc(&ctl[5], lane);
      unsigned t0;
      ORG(t0);
      const f32x4 m = result(mk((unsigned char*)ARG(ka, 3), WS_END), WS_LB, t0 + P0 + 3 * pd / 4 + SRD, ta, tb, lane);
      const int sl = (m.y - m.x + m.z + fminf(fmaxf(0.4f * (m.x - 66.f), 0.f), 2.5f) > 0.5f * (float)THK || m.x > 500.f) ? 1 : 0, lv = min(max((int)m.x - LBK, 0) * LB0 / 8, 40);
      ctl[6] = lv; ctl[7] = sl * SLK; ctl[25] = lv + 4 * sl * SLK;
    }
    KPACE(w < 2 ? QM + w : w)
    LDK()
    PIN();
    lds_inc(&ctl[4], lane);
  }
#define F_(x) __uint_as_float(x)
  ARGS2();
  asm volatile("" : "+s"(w));
  unsigned char* ws = (unsigned char*)ARG(ka, 3);
  const u16* wo = (const u16*)ARG(kb, 1); const u16* w1 = (const u16*)ARG(kb, 2); const u16* w3 = (const u16*)ARG(kb, 3);
  const u16* w2 = (const u16*)ARG(kc4, 0); u16* out = (u16*)ARG(kc4, 1);
  const rsrc_t rl = mk(ws, WS_END);
  const int pd = one ? RV : RV2;
  const int dw = w & 1, csw = (c8 ^ (r8 & 2) ^ (4 * dw)) * 16;
  {
    if (w == 0) {
      __builtin_amdgcn_s_setprio(3);
      u32x4 qv[5], ov[1];
      int qo = WS_PQ + h * 5120 + lane * 16, oo = WS_TO + c8 * 128;
#pragma unroll 1
      for (unsigned spins = 0; spins < SPIN_CAP; ++spins) {
        asm volatile("" : "+v"(qo), "+v"(oo) :: "memory");
        load_units<5, 1024, SC1 | NT>(rl, qo, qv);
        load_units<1, 0>(rl, oo, ov);
        if (none(units_bad<1>(ov, ta, tb, units_bad<5>(qv, ta, tb)) != 0u)) break;
      }
      f32x2 v[5];
#pragma unroll
      for (int k = 0; k < 5; ++k) { const unsigned u0 = qv[k].x, u1 = qv[k].z; v[k] = f32x2{__uint_as_float(u0), __uint_as_float(u1)}; }
      const bool hi = lane >= 32;
      const f32x2 c0 = hi ? v[0] : v[0] + v[1], c1 = hi ? v[1] + v[2] : v[2] + v[3], c2 = hi ? v[3] + v[4] : v[4];
      qs[lane] = pk2(sw32(c0.x, c1.x), sw32(c0.y, c1.y));
      qs[64 + (lane & 31)] = pk2(sw32(c2.x, c2.x), sw32(c2.y, c2.y));
      const unsigned o_ = ov[0].x, o0 = __builtin_amdgcn_readfirstlane(o_);
      float od = (float)(int)(o_ - o0);
      od += dppf<XOR1>(od); od += dppf<XOR2>(od); od += dppf<HMIR>(od);
      const unsigned p_ = ov[0].z;
      float vt = __uint_as_float(p_);
      vt += dppf<XOR1>(vt); vt += dppf<XOR2>(vt); vt += dppf<HMIR>(vt);
      asm volatile("s_waitcnt lgkmcnt(0)" ::: "memory");
      ctl[3] = ((o0 + (unsigned)(int)(od * 0.125f)) & ~3u) | (vt > 0.f ? 3u : 1u);
    }
  }
  __builtin_amdgcn_s_setprio(1);
  __builtin_amdgcn_s_waitcnt(0x0F70);
  asm volatile("" :: "v"(W[12]), "v"(W[13]), "v"(W[14]), "v"(xr[0]), "v"(xr[1]), "v"(xr[2]), "v"(xr[3]), "v"(xr[4]));
  unsigned tc;
  int pol;
  float wm;
  {
    const rsrc_t rv = mk((const void*)ARG(kb, 0), H * SK * DH * 2);
    const int vv = r8 * 384 + csw;
#define SH(n) (lds_ld(&ctl[25]) + (n) * lds_ld(&ctl[8]) * (one ? 8 : 7) / 32)
#define SHC() (lds_ld(&ctl[25]) + 15 * lds_ld(&ctl[8]) / 4)
#define LDV(vg) { WAITC(4, NK + 8 * (vg)); \
    int so_ = BLK(4 * (vg)) + dw * 3072; unsigned ld_ = lb + L_RING + ((vg) == 3 ? 0 : (vg)) * 49152 + dw * 3072; \
    _Pragma("unroll 1") for (int i = 4 * (vg); i < 4 * (vg) + 4; ++i) { PACE(DUE(i) - 8) PACE(DUE(i) + SH(i)) \
      dma3(rv, vv, so_, ld_); dma3(rv, vv, so_ + 6144, ld_ + 6144); so_ += 98304; ld_ += 12288; lds_inc(&ctl[4], lane); } \
    }
    unsigned tg;
    while ((tg = lds_ld(&ctl[3])) == 0u) __builtin_amdgcn_s_sleep(2);
    asm volatile("" ::: "memory");
    pol = (tg & 2u) << 12;
    const unsigned to = lds_ld(&ctl[30]);
    tg = to + min(max((int)(tg - to), -300), 300);
    tz = tg + SHIFT(h, (unsigned)RK, tg) + P0 + GK + 4 * RK;
    tc = tg + P0 + GK + 4 * RK + 15 * RV / 4;
    ta = __builtin_amdgcn_readfirstlane(ctl[0]); tb = __builtin_amdgcn_readfirstlane(ctl[1]);
    float pe;
    {
      const unsigned* qp = &qs[4 * c8];
      u32x4 qf[3];
#pragma unroll
      for (int jj = 0; jj < 3; ++jj) qf[jj] = *reinterpret_cast<const u32x4*>(qp + 32 * jj);
      float d[4];
#pragma unroll
      for (int g = 0; g < 4; ++g) {
        d[g] = 0.f;
#pragma unroll
        for (int jj = 0; jj < 3; ++jj)
#pragma unroll
          for (int i = 0; i < 4; ++i) d[g] = dot2(K[3 * g + jj][i], qf[jj][i], d[g]);
        d[g] += dppf<HMIR>(d[g]);
      }
      const float e0 = rs<XOR2>(d[0], d[2], lane & 2), e1 = rs<XOR2>(d[1], d[3], lane & 2);
      float sc = rs<XOR1>(e0, e1, lane & 1);
      sc = bfr(bfr(sc) * 0.07216878364870322f);
      wm = max64(sc);
      pe = sc;
      if (lane == 0) wmax[w] = wm;
      lds_inc(&ctl[9], lane);
    }
    const int vg = (0x42144403 >> (4 * (w >> 1))) & 15;
    if (vg < 2) LDV(vg)
    if (w == 9) survey(rl, WS_LB + 4096, SLOT(), &ctl[4], NK + 2u, 2u, DUE(0) + lds_ld(&ctl[25]), pd, ta, tb, lane);
    while (lds_ld(&ctl[9]) < NST) __builtin_amdgcn_s_sleep(2);
    asm volatile("" ::: "memory");
    u16* const pp = reinterpret_cast<u16*>(smem + L_P) + 32 * pw + 8 * (lane & 3) + r8;
    wm = max16(wmax[lane & 15]);
    pe = __builtin_amdgcn_exp2f((pe - wm) * L2E);
    {
      const float p = bfr(pe);
      *pp = (u16)(__float_as_uint(p) >> 16);
      const float ls = red64(p) * 0.5f;
      if (lane == 0) lsum[w] = ls;
    }
    lds_inc(&ctl[10], lane);
    if (w >= 2) lds_inc(&ctl[16 + (pw >> 2)], lane);
    if (vg == 3) {
      WAITC(10, NST);
      *pp = (u16)pk2(pe * __builtin_amdgcn_rcpf(red16(lsum[lane & 15])), 0.f);
      WAITC(20, 6u);
    }
    if (vg < 4) {
      if (vg >= 2) LDV(vg)
      if (vg == 3) VMW(12); else VMW(0);
      lds_inc(&ctl[16 + vg], lane);
    }
  }
  const bool role4 = one, pvw = w >= 4 && w < 10;
  const int q3 = j & 3;
  const int ho = ((!role4 && q3 == 3) ? xcd : xcd ^ 1) + 8 * (lt ? j >> 3 : (j >> 2) & 3), ob = lt ? j & 7 : role4 ? q3 : (q3 == 3 ? 8 : 4 + 2 * q3);
  const int rg = j < 24 ? xcd : xcd ^ 1, ln = j < 24 ? j : j - 8, n5 = 12 * (xcd >> 1) + j - 16;
  const int g4 = lane >> 4, tq = (lane >> 2) & 3, tp = lane & 3;
#define FRAG(t) (g4 * 1024 + tq * 128 + (((2 * (t) + (tp >> 1)) ^ (tq & 2) ^ (4 * (g4 & 1))) * 16) + (tp & 1) * 8)
  const int vo = r8 * (D * 2) + csw;
  const unsigned n12 = (role4 ? 4u : 8u);
  if (pvw) {
    const int t2 = w - 4;
    unsigned va = lb + L_RING + 2 * g4 * 1024 + (t2 >> 1) * 1024 + FRAG(2 * (t2 & 1)), vb = va ^ 32u, pa = lb + L_P + 16 * g4;
    f32x4 ac0 = {0.f, 0.f, 0.f, 0.f}, ac1 = {0.f, 0.f, 0.f, 0.f};
#define PVB(i) { const u32x4 pf = lds_ld4(pa, 64 * (i)); \
    const u32x4 a0 = frag(va, ((i) & 3) * 12288, ((i) & 3) * 12288 + 512), a1 = frag(vb, ((i) & 3) * 12288, ((i) & 3) * 12288 + 512); \
    ac0 = mma(a0, pf, ac0); ac1 = mma(a1, pf, ac1); }
#define PVD() PINV(ac0); PINV(ac1); PIN();
    WAITC(16, 6u);
    PVB(0) PVB(1) PVB(2) PVB(3) va += 49152; vb += 49152;
    lds_inc(&ctl[20], lane);
    PVD()
    WAITC(17, 6u);
    PVB(4) PVB(5) PVB(6) PVB(7) va += 49152; vb += 49152;
    lds_inc(&ctl[21], lane);
    PVD()
    WAITC(10, NST);
    float rl_ = __builtin_amdgcn_rcpf(red16(lsum[lane & 15]));
    PINV(rl_);
    WAITC(18, 6u);
    PVB(8) PVB(9) PVB(10) PVB(11) va -= 98304; vb -= 98304;
    lds_inc(&ctl[22], lane);
    PVD()
    __builtin_amdgcn_s_setprio(3);
    SPINC(19, 4u);
    PVB(12) PVB(13)
    ac0 *= rl_; ac1 *= rl_;
    u32x4 pf = lds_ld4(pa, 64 * 14), pg = lds_ld4(pa, 64 * 15);
    int p1off = (lane & 14) ? OOB : (at_rec(h) ^ pol) + s * 1024 + (8 * t2 + 4 * (lane & 1) + g4) * 16;
    PINV(ac0); PINV(ac1); PINV(pf); PINV(pg); PINV(p1off); PINV(va); PINV(vb);
    SPINC(23, 2u);
    const u32x4 a0 = frag(va, 2 * 12288, 2 * 12288 + 512), a1 = frag(vb, 2 * 12288, 2 * 12288 + 512);
    const u32x4 a2 = frag(va, 3 * 12288, 3 * 12288 + 512), a3 = frag(vb, 3 * 12288, 3 * 12288 + 512);
    PIN();
    ac0 = mma(a0, pf, ac0); ac1 = mma(a1, pf, ac1);
    ac0 = mma(a2, pg, ac0); ac1 = mma(a3, pg, ac1);
    const unsigned x0 = pk2(ac0.x, ac0.y), z0 = pk2(ac0.z, ac0.w), x1 = pk2(ac1.x, ac1.y), z1 = pk2(ac1.z, ac1.w);
    put_unit(rl, p1off, __uint_as_float((lane & 1) ? x1 : x0), __uint_as_float((lane & 1) ? z1 : z0), ta, tb);
  } else if (w < 2) {
    __builtin_amdgcn_s_setprio(3);
    VMW(0);
    lds_inc(&ctl[23], lane);
    tc += SHC();
    const int lag = (int)((unsigned)__builtin_amdgcn_s_memrealtime() - tc) - TY;
    if (!w) ctl[26] = ((tc + (lag > 0 ? lag : 0)) | 1u);
    BAR();
  } else if (w < 4) {
    const rsrc_t ro = mk(wo, H * DH * D * 2);
    const int so_o = ((ho * DH + 8 * dw) * D + 64 * ob) * 2;
    WAITS(21, 6u);
    WAITS(4, NK + 28);
    WAITC(4, NK + 32);
    const unsigned t0 = role4 ? tc + TW + SHC() : DUE(16) + SH(16), dt = role4 ? WQD : pd / 8;
    __builtin_amdgcn_s_setprio(3);
#pragma unroll 1
    for (int q = 0; q < (role4 ? 4 : 8); ++q) {
      until(t0 + q * dt);
#pragma unroll
      for (int n = 0; n < 3; ++n)
        dma1(ro, vo, so_o + (q >> 2) * 128 + ((q & 3) * 3 + n) * 16 * D * 2, lb + L_WO + (2 * (3 * q + n) + dw) * 1024);
    }
    VMW(3);
    lds_inc(&ctl[28], lane);
    VMW(0);
    BAR0();
  } else if (w == 14) {
    const unsigned s0 = lds_ld(&ctl[25]);
    const f32x4 m = result(rl, WS_LB + 4096, tc - 3 * RV + s0 + SRV, ta, tb, lane);
    JUDGE(m, 0.5f * (float)THV, sl, lv);
    ctl[25] = s0 + lv; ctl[8] = sl * SLV;
  } else if (w == 11) {
    WAITS(10, NST);
    const float lse = fmaf(wm, L2E, __builtin_amdgcn_logf(red16(lsum[lane & 15])));
    WAITS(18, 6u);
    tc += SHC();
    PACE(tc)
    put_unit(rl, lane < 8 ? (at_rec(h) ^ pol) + s * 1024 + 768 + lane * 16 : OOB, lse, lse, ta, tb);
    PACE(tc + TL)
    u32x4 lv_[1];
    poll_units<1, 0>(rl, (at_rec(ho) ^ pol) + c8 * 1024 + 768, lv_, ta, tb);
    const unsigned mu = lv_[0].x;
    const float ms = __uint_as_float(mu);
    float Mg = fmaxf(ms, dppf<XOR1>(ms)); Mg = fmaxf(Mg, dppf<XOR2>(Mg)); Mg = fmaxf(Mg, dppf<HMIR>(Mg));
    const float wg = __builtin_amdgcn_exp2f(ms - Mg);
    float Lg = wg + dppf<XOR1>(wg); Lg += dppf<XOR2>(Lg); Lg += dppf<HMIR>(Lg);
    lds_st(lb + L_WRE + 4 * c8, __float_as_uint(wg * __builtin_amdgcn_rcpf(Lg)));
    lds_inc(&ctl[28], lane);
  }
  __builtin_amdgcn_s_setprio(3);
  if (w >= (role4 ? 12 : 8)) {
    const int bk = w < 12;
    unsigned ba = lb + L_WO + bk * 24576 + FRAG(w & 3);
    unsigned ao = lb + L_REC + c8 * 416 + 16 * g4;
    int so = lane < 4 ? WS_P2 + (at_row(ho >> 1, 4 * (ob + bk) + (w & 3)) ^ pol) + (ho & 1) * 64 + lane * 16 : OOB;
    int ro_ = lane < 48 ? (at_rec(ho) ^ pol) + (w & 3) * 2048 + lane * 16 : OOB;
    unsigned ra = lb + L_REC + (w & 3) * 832 + (lane < 48 ? lane * 8 : 384);
    unsigned tl = lane < 48 ? ta : 0u, th = lane < 48 ? tb : 0u;
    PINV(ba); PINV(ao); PINV(so); PINV(ro_); PINV(ra); PINV(tl); PINV(th);
    u32x4 bf[6], af[6], v[2], wu;
    if (!bk) {
      WAITS(26, 1u);
      until(lds_ld(&ctl[26]) + TB);
      asm volatile("buffer_load_dwordx4 %0, %2, %3, 0 offen sc1\n\tbuffer_load_dwordx4 %1, %2, %3, 0 offen offset:1024 sc1" : "=&v"(v[0]), "=&v"(v[1]) : "v"(ro_), "s"(rl) : "memory");
#define AHEAD() SPINC(28, 3u);                 \
      _Pragma("unroll") for (int ks = 0; ks < 4; ++ks) bf[ks] = frag(ba, ks * 4096, ks * 4096 + 512); \
      wu = lds_ld4(lb + L_WRE + 16 * (g4 & 1), 0); \
      _Pragma("unroll") for (int ks = 0; ks < 4; ++ks) PINV(bf[ks]); \
      PINV(wu); PIN();
      AHEAD()
      asm volatile("s_waitcnt vmcnt(0)" : "+v"(v[0]), "+v"(v[1]) :: "memory");
#define RECS() _Pragma("unroll") for (int k = 0; k < 2; ++k) *(__attribute__((address_space(3))) u32x2*)(size_t)(ra + k * 416) = u32x2{v[k].x, v[k].z};
      RECS()
      if (RARE(!none(units_bad<2>(v, tl, th) != 0u))) {
        poll_lane<2, 1024>(rl, ro_, v, tl, th);
        RECS()
      }
    } else {
      WAITS(28, 1u);
      AHEAD()
    }
    BAR();
    const f32x4 wv = __builtin_bit_cast(f32x4, wu);
#pragma unroll
    for (int ks = 0; ks < 4; ++ks) af[ks] = lds_ld4(ao, 64 * ks);
#pragma unroll
    for (int ks = 4; ks < 6; ++ks) { af[ks] = lds_ld4(ao, 64 * ks); bf[ks] = frag(ba, ks * 4096, ks * 4096 + 512); }
    PIN();
    f32x4 acc = {0.f, 0.f, 0.f, 0.f}, ac2 = {0.f, 0.f, 0.f, 0.f};
#pragma unroll
    for (int ks = 0; ks < 6; ks += 2) { acc = mma(af[ks], bf[ks], acc); ac2 = mma(af[ks + 1], bf[ks + 1], ac2); }
    float c0 = fmaf(wv.w, acc.w, fmaf(wv.z, acc.z, fmaf(wv.y, acc.y, wv.x * acc.x)));
    const float c2 = fmaf(wv.y, ac2.y, wv.x * ac2.x), c3 = fmaf(wv.w, ac2.w, fmaf(wv.z, ac2.z, c0));
    c0 = c2 + c3;
    c0 = sw16(c0, c0);
    const float c1 = __uint_as_float(pk2(c0, dppf<0x104>(c0)));
    put_unit(rl, so, c1, dppf<ROR8>(c1), ta, tb);
    PIN();
    lds_inc(&ctl[12], lane);
  }
  if (role4) {
    if ((0x0E0C >> w) & 1) {
      if (w >= 4) BAR0();
      const int hw = w < 4 ? w - 2 : w - 7;
      const int cl = 16 * hw + (lane & 3) + 8 * (lane >> 5) + ((lane >> 2) & 4);
      float xv = lo16(reinterpret_cast<const u16*>(xraw)[80 * rg + cl]);
      unsigned hoa = lb + L_HS + 2 * cl;
      u32x4 v[2];
      int po = WS_P2 + (at_row(r8, 5 * rg + hw) ^ pol) + c8 * 16;
      PINV(xv); PINV(hoa); PINV(po);
      const unsigned th_ = (lds_ld(&ctl[26]) + 100 + TD);
      __builtin_amdgcn_s_sleep(2);
      until(th_);
      load_units<2, 65536>(rl, po, v);
#define HSUM() { const unsigned a0 = v[0].x, a1 = v[0].z, b0 = v[1].x, b1 = v[1].z; \
      const float s0 = sw32(lo16(a0) + lo16(b0), lo16(a1) + lo16(b1)), s1 = sw32(hi16(a0) + hi16(b0), hi16(a1) + hi16(b1));      \
      float sm = sw16(s0, s1);                          \
      sm += dppf<ROR8>(sm); sm += dppf<0x124>(sm);      \
      sm = bfr(sm) + xv; \
      *(__attribute__((address_space(3))) u16*)(size_t)hoa = (u16)pk2(sm, sm); }
      HSUM()
      if (!none(units_bad<2>(v, ta, tb) != 0u)) {
        poll_lane<2, 65536>(rl, po, v, ta, tb);
        HSUM()
      }
      BAR();
    } else if (w < 13) {
      if (w >= 4 && w < 9) {
        const rsrc_t r1 = mk(w1, D * F * 2), r3 = mk(w3, D * F * 2);
        const int q = w - 4;
        const int v0 = r8 * (F * 2) + csw, v1 = v0 ^ 64, so = ((80 * rg + 8 * q) * F + ln * 64) * 2;
        const unsigned fd = lb + L_FF + q * 1024;
        WAITC(22, 6u);
        WAITS(26, 1u);
        until(lds_ld(&ctl[26]) + (q < NE ? F1S : TB + F1D));
        dma1(r1, v0, so, fd); dma1(r3, v0, so, fd + 10240);
        dma1(r1, v1, so + 40 * F * 2, fd + 5120); dma1(r3, v1, so + 40 * F * 2, fd + 15360);
        BAR0();
        VMW(0);
        lds_inc(&ctl[13], lane);
      } else if (w < 2) {
        WAITC(12, n12);
        PACE(lds_ld(&ctl[26]) + 40 + TD + PW)
        put_unit(rl, (at_g(2 * ln + w) ^ pol) + (rg * 64 + lane) * 16, 0.f, 0.f, ~ta, ~tb);
      }
      const int tw = w < 2 ? w : w < 9 ? w - 2 : 7;
      unsigned bo = lb + L_FF + (tw >> 2) * 10240 + FRAG(tw & 3), b2 = bo + (g4 < 2 ? 8192 : 0);
      int go4 = lane < 16 ? (at_g(2 * ln + ((tw >> 1) & 1)) ^ pol) + (rg * 64 + (tw >> 2) * 32 + (tw & 1) * 16 + lane) * 16 : OOB;
      unsigned ao = lb + L_HS + 192 * (lane & 1) + 16 * g4;
      PINV(go4); PINV(ao); PINV(bo); PINV(b2);
      u32x4 af[3], bf[3];
      WAITC(13, 5u);
      bf[0] = frag(bo, 0, 512); bf[1] = frag(bo, 4096, 4096 + 512); bf[2] = frag(b2, 0, 512);
      PINV(bf[0]); PINV(bf[1]); PINV(bf[2]);
      VMW(0);
      BAR0();
#pragma unroll
      for (int ks = 0; ks < 3; ++ks) af[ks] = lds_ld4(ao, 64 * ks);
      PIN();
      f32x4 acc = {0.f, 0.f, 0.f, 0.f};
#pragma unroll
      for (int ks = 0; ks < 3; ++ks) acc = mma(af[ks], bf[ks], acc);
      put_unit(rl, go4, acc.x, acc.y, ta, tb);
    } else if (w == 13) {
      unsigned char* go = ws + (at_m3(2 * ln + (lane & 1), rg) ^ pol);
      unsigned mt = ta;
      PINV(go); PINV(mt);
      BAR0();
      RING(go, mt, 3ull);
      __builtin_amdgcn_raw_buffer_store_b64(u32x2{ta, tb ^ 0x9E3779B9u}, rl, (b == 0 && lane == 0) ? 0 : OOB, 0, SC1);
    } else if (w == 14) {
      unsigned hi_ = lb + L_HS + 4 * (lane < 40 ? lane : 0);
      int sto = lane < 8 ? (at_st(ln) ^ pol) + rg * 128 + lane * 16 : OOB;
      PINV(hi_); PINV(sto);
      PACE(lds_ld(&ctl[26]) + 40 + TD + PW)
      put_unit(rl, sto, 0.f, 0.f, ~ta, ~tb);
      VMW(0);
      BAR0();
      const unsigned hp = *(lds_u32)(size_t)hi_;
      const float h0 = lane < 40 ? lo16(hp) : 0.f, h1 = lane < 40 ? hi16(hp) : 0.f;
      const float cs = __uint_as_float(__builtin_amdgcn_readfirstlane(hp) << 16);
      const float d0 = lane < 40 ? h0 - cs : 0.f, d1 = lane < 40 ? h1 - cs : 0.f;
      float sr = red32(sw32(d0 + d1, d0 * d0 + d1 * d1));
      const float s2 = __uint_as_float(__builtin_amdgcn_readlane(__float_as_uint(sr), 32));
      const float mw = fmaf(sr, 1.f / 80.f, cs), m2 = fmaxf(fmaf(-sr * (1.f / 80.f), sr, s2), 0.f);
      put_unit(rl, sto, mw, m2, ta, tb);
    } else if (j < 5) {
      const rsrc_t rout = mk(out, (D * 2));
      const int cl = 16 * j + c8 + 8 * (lane >> 5);
      unsigned char* gi = ws + (at_m4(5 * (xcd >> 2) + j, lane < N5 ? lane : 0) ^ pol);
      int op = (lane & 24) ? OOB : (80 * xcd + cl) * 2;
      int po = WS_P5 + (at_row(r8, 5 * xcd + j) ^ pol) + c8 * 16;
      unsigned sl[4];
      PINV(gi); PINV(op); PINV(po);
      BAR0();
      float hv = lo16(reinterpret_cast<const u16*>(hs)[cl]);
      PINV(hv);
      PACE(NOW() + FS)
      until(gate(gi, ta, (1ull << N5) - 1ull, sl) + D4);
      u32x4 v[6];
      load_units<6, 65536>(rl, po, v);
      PIN();
#define FIN_() { float s0 = F_(v[0].x), s1 = F_(v[0].z); bad = 0u; \
        _Pragma("unroll") for (int k = 0; k < 6; ++k) { if (k) { s0 += F_(v[k].x); s1 += F_(v[k].z); } \
          if (k < 5) { bad = __builtin_amdgcn_bitop3_b32(v[k].y, ta, bad, 0xBE); bad = __builtin_amdgcn_bitop3_b32(v[k].w, tb, bad, 0xBE); PINV(s0); PINV(s1); PINV(bad); PIN(); } } \
        s0 = sw32(s0, s1); \
        bad = __builtin_amdgcn_bitop3_b32(v[5].y, ta, bad, 0xBE); bad = __builtin_amdgcn_bitop3_b32(v[5].w, tb, bad, 0xBE); \
        s0 = sw16(s0, s0); s0 += dppf<ROR8>(s0); s0 = bfr(s0) + hv; res = pk2(s0, s0); }
      unsigned res, bad;
      FIN_()
      PINV(res);
      if (RARE(!none(bad != 0u))) {
        poll_units<6, 65536>(rl, po, v, ta, tb);
        FIN_()
      }
      __builtin_amdgcn_raw_buffer_store_b16((u16)res, rout, op, 0, 0);
      KEEP(sl);
      asm volatile("s_endpgm");
    }
  } else if (j < 28) {
    if (w == 3) {
      unsigned char* gi = ws + (at_m3(n5, c8) ^ pol);
      unsigned ai = lb + L_ACT + (lane < 32 ? 2 * lane : 64), fa = lb + L_CTL + 24 * 4;
      int go_ = (at_g(n5) ^ pol) + lane * 16, so_ = (at_st(n5 >> 1) ^ pol) + c8 * 128;
      unsigned sl[4], lvv = 1u;
      PINV(gi); PINV(ai); PINV(fa); PINV(go_); PINV(so_); PINV(lvv);
      WAITC(12, n12);
      wait_marks(gi, ta, 0xFFull, sl);
      u32x4 gv[8], sv[1];
      load_units<1, 0>(rl, so_, sv);
      load_units<8, 1024>(rl, go_, gv);
      PIN();
      lds_st(fa, lvv);
      PIN();
#define ACT_() { const float c0s = F_(__builtin_amdgcn_readfirstlane(sv[0].x));       \
        float da = F_(sv[0].x) - c0s, m2 = fmaf(80.f * da, da, F_(sv[0].z)); \
        da += dppf<XOR1>(da); m2 += dppf<XOR1>(m2); da += dppf<XOR2>(da); m2 += dppf<XOR2>(m2); da += dppf<HMIR>(da); m2 += dppf<HMIR>(m2); \
        da *= 0.125f; \
        const float var = bfr(fmaxf(fmaf(-da, da, m2 * (1.f / D)), 0.f)); \
        float mb = bfr(c0s + da), rd = __builtin_amdgcn_rcpf(bfr(__builtin_amdgcn_sqrtf(bfr(var + 1e-5f)))); \
        PINV(mb); PINV(rd); PIN(); \
        float gs_ = F_(gv[0].x), cs_ = F_(gv[0].z); \
        bad = __builtin_amdgcn_bitop3_b32(sv[0].y, ta, 0u, 0xBE); bad = __builtin_amdgcn_bitop3_b32(sv[0].w, tb, bad, 0xBE); \
        _Pragma("unroll") for (int k = 0; k < 8; ++k) { if (k) { gs_ += F_(gv[k].x); cs_ += F_(gv[k].z); } \
          if (k < 7) { bad = __builtin_amdgcn_bitop3_b32(gv[k].y, ta, bad, 0xBE); bad = __builtin_amdgcn_bitop3_b32(gv[k].w, tb, bad, 0xBE); PINV(gs_); PINV(cs_); PINV(bad); PIN(); } } \
        float val = bfr((gs_ - mb * cs_) * rd); \
        float sg = bfr(val * __builtin_amdgcn_rcpf(1.f + __builtin_amdgcn_exp2f(-val * L2E))); \
        const float av = sg * F_(__builtin_amdgcn_permlane32_swap(__float_as_uint(val), __float_as_uint(val), false, false)[1]);     \
        *(__attribute__((address_space(3))) u16*)(size_t)ai = (u16)pk2(av, av); \
        bad = __builtin_amdgcn_bitop3_b32(gv[7].y, ta, bad, 0xBE); bad = __builtin_amdgcn_bitop3_b32(gv[7].w, tb, bad, 0xBE); }
      unsigned bad;
      ACT_()
      if (RARE(!none(bad != 0u))) {
        poll_units<8, 1024>(rl, go_, gv, ta, tb); poll_units<1, 0>(rl, so_, sv, ta, tb);
        ACT_()
      }
      BAR();
      KEEP(sl);
    } else if (w == 2) {
      unsigned char* go = ws + (at_m4(lane < NG4 ? lane : 0, n5) ^ pol);
      PINV(go);
      WAITC(13, 4u);
      SPINC(24, 1u);
      announce(go, ta, (unsigned)__builtin_amdgcn_s_memrealtime(), ((1ull << NG4) - 1ull));
      BAR0();
    } else if (w >= 4 && w < 14) {
      if (w < 8) {
        const rsrc_t r2 = mk(w2, F * D * 2);
        const int q = w - 4, so = (R5 * n5 + 8 * q) * D * 2;
        const unsigned fd = lb + L_FF + q * 1024;
        WAITC(22, 6u);
        WAITC(26, 1u);
        until(lds_ld(&ctl[26]) + TB + W2T);
#pragma unroll 1
        for (int i = 0; i < 10; ++i) {
          if (i == W2B) { BAR0(); WAITC(12, n12); }
          dma1(r2, vo, so + i * 128, fd + i * 4096);
        }
        VMW(0);
        lds_inc(&ctl[13], lane);
      }
      const int fw = w - 4;
      int so = (lane & 8) ? OOB : WS_P5 + (at_row(n5, 4 * fw) ^ pol) + (8 * g4 + c8) * 16;
      WAITC(13, 4u);
      u32x4 bf[4];
#pragma unroll
      for (int t = 0; t < 4; ++t) bf[t] = frag(lb + L_FF + fw * 4096 + FRAG(t), 0, 512);
#pragma unroll
      for (int t = 0; t < 4; ++t) asm volatile("" : "+v"(bf[t]));
      unsigned ao = lb + L_ACT + 16 * g4;
      PINV(ao); PINV(so);
      BAR0();
      const u32x4 av = lds_ld4(ao, 0);
      const f32x4 z = {0.f, 0.f, 0.f, 0.f};
      f32x4 d0 = mma(av, bf[0], z), d1 = mma(av, bf[1], z), d2 = mma(av, bf[2], z), d3 = mma(av, bf[3], z);
      asm volatile("" : "+v"(d0), "+v"(d1), "+v"(d2), "+v"(d3));
      float c0 = __uint_as_float(__builtin_amdgcn_update_dpp(__float_as_uint(d0.x), __float_as_uint(d1.x), 0xE4, 0x2, 0xf, false));
      c0 = __uint_as_float(__builtin_amdgcn_update_dpp(__float_as_uint(c0), __float_as_uint(d2.x), 0xE4, 0x4, 0xf, false));
      c0 = __uint_as_float(__builtin_amdgcn_update_dpp(__float_as_uint(c0), __float_as_uint(d3.x), 0xE4, 0x8, 0xf, false));
      const float c1 = dppf<ROR8>(c0);
      put_unit(rl, so, c0, c1, ta, tb);
      PIN();
    }
  }
}
}

extern "C" void kernel_launch(void* const* d_in, const int* in_sizes, int n_in, void* d_out, int out_size, void* d_ws, size_t ws_size,
                              hipStream_t stream) {
  hipLaunchKernelGGL(k_fused, dim3(NWG), dim3(NTHR), 0, stream, (const u16*)d_in[0], (const u16*)d_in[1], (const u16*)d_in[3], (unsigned char*)d_ws,
                     (const u16*)d_in[2], (const u16*)d_in[4], (const u16*)d_in[6], (const u16*)d_in[7], (const u16*)d_in[8], (u16*)d_out);
}
```
